```python
import torch

def _softmax(x, axis=-1):
    return torch.softmax(x, dim=axis)
def _gelu(x):
    return torch.nn.functional.gelu(x, approximate="tanh")
def _silu(x):
    return torch.nn.functional.silu(x)
def _sigmoid(x):
    return torch.sigmoid(x)
def _layernorm(x, axis=-1, eps=1e-5):
    m = x.mean(dim=axis, keepdim=True)
    v = x.var(dim=axis, keepdim=True, unbiased=False)
    return (x - m) / torch.sqrt(v + eps)
def _scatter_add(tgt, idx, src, axis=0):
    return tgt.clone().index_add_(axis, idx, src)
def _segment_sum(src, seg, n):
    out = torch.zeros((n,) + src.shape[1:], dtype=src.dtype)
    return out.index_add_(0, seg, src)
def _pad(x, pw):
    flat = tuple(v for pair in reversed(pw) for v in pair)
    return torch.nn.functional.pad(x, flat)

kernel_name = 'sequential_preLN__linear_attn_block__mixer_channel_mlp__prompt'

def setup_inputs():

    _w = (lambda a: (lambda t: t.to(torch.bfloat16) if t.is_floating_point() else t)(torch.as_tensor(a)))
    def _A_setup():
        B, S, D = 1, 2048, 512
        return {
            "x": _w((lambda *s: torch.randn(*s, dtype=torch.bfloat16) * 0.05)(B, S, D)),
            "wqkv": _w((lambda *s: torch.randn(*s, dtype=torch.bfloat16) * 0.1)(D, 3 * D)),
            "wo": _w((lambda *s: torch.randn(*s, dtype=torch.bfloat16) * 0.05)(D, D)),
        }
    _A_in = _A_setup()

    def _B_setup():
        B, S, D, F = 1, 2048, 512, 1792
        return {
            "x": _w((lambda *s: torch.randn(*s, dtype=torch.bfloat16) * 0.05)(B, S, D)),
            "w1": _w((lambda *s: torch.randn(*s, dtype=torch.bfloat16) * 3.2)(D, F) * (D ** -0.5)),
            "b1": _w((lambda *s: torch.randn(*s, dtype=torch.bfloat16) * 0.8)(F)),
            "w2": _w((lambda *s: torch.randn(*s, dtype=torch.bfloat16) * 0.05)(F, D) * (F ** -0.5)),
            "b2": _w((lambda *s: torch.randn(*s, dtype=torch.bfloat16) * 0.05)(D)),
        }
    _B_in = _B_setup()

    return {
        "A_x": _A_in["x"],
        "A_wqkv": _A_in["wqkv"],
        "A_wo": _A_in["wo"],
        "B_x": _B_in["x"],
        "B_w1": _B_in["w1"],
        "B_b1": _B_in["b1"],
        "B_w2": _B_in["w2"],
        "B_b2": _B_in["b2"],
    }

def reference(A_x, A_wqkv, A_wo, B_x, B_w1, B_b1, B_w2, B_b2):
    def _A_ref(x, wqkv, wo):
        B, S, D = x.shape
        H = 32; Dh = D // H
        qkv = (lambda x, *d: x.permute(*d))((x @ wqkv).reshape(B, S, 3, H, Dh), 2, 0, 3, 1, 4)
        q, k, v = qkv[0], qkv[1], qkv[2]
        phi = lambda t: torch.where(t > 0, t + 1, torch.exp(t)) + 1e-6
        pq, pk = phi(q), phi(k)
        kv = torch.einsum("bhsd,bhse->bhde", pk, v)
        z = torch.einsum("bhsd->bhd", pk)
        num = torch.einsum("bhqd,bhde->bhqe", pq, kv)
        den = torch.einsum("bhqd,bhd->bhq", pq, z)[..., None] + 1e-6
        o = num / den
        return (lambda x, *d: x.permute(*d))(o, 0, 2, 1, 3).reshape(B, S, D) @ wo

    def _B_ref(x, w1, b1, w2, b2):
        y = _layernorm(x, axis=-1)
        h = _gelu(torch.einsum("bsd,df->bsf", y, w1) + b1)
        return torch.einsum("bsf,fd->bsd", h, w2) + b2

    x = A_x
    h = x + _A_ref(_layernorm(x, axis=-1), A_wqkv, A_wo)
    h2 = h + _B_ref(_layernorm(h, axis=-1), B_w1, B_b1, B_w2, B_b2)
    return h2
```

```cpp
#include <hip/hip_runtime.h>
#include <hip/hip_bf16.h>
#include <stdint.h>

typedef __attribute__((ext_vector_type(8))) short bf16x8;
typedef __attribute__((ext_vector_type(4))) short bf16x4;
typedef __attribute__((ext_vector_type(4))) short v4i16_t;
typedef __attribute__((ext_vector_type(4))) float f32x4;
typedef __attribute__((ext_vector_type(2))) float f32x2;
typedef __attribute__((ext_vector_type(4))) unsigned u32x4;
typedef __attribute__((ext_vector_type(2))) unsigned u32x2;
typedef __attribute__((ext_vector_type(2))) __bf16 bfv2;

#define DEV __device__ __forceinline__
#define LDS_AS __attribute__((address_space(3)))

static constexpr size_t WS_PQ  = 0;
static constexpr size_t WS_H   = WS_PQ + (2u << 20);
static constexpr size_t WS_HM  = WS_H + (2u << 20);
static constexpr size_t WS_WO  = WS_HM + (7u << 20);
static constexpr size_t WS_W1  = WS_WO + (512u << 10);
static constexpr size_t WS_W2  = WS_W1 + (1792u << 10);
static constexpr size_t WS_KVP = WS_W2 + (1792u << 10);
static constexpr size_t WS_ZP  = WS_KVP + (512u << 10);
static constexpr size_t WS_KVF = WS_ZP + (32u << 10);
static constexpr size_t WS_ZF  = WS_KVF + (16u << 10);
static constexpr size_t WS_CS1 = WS_ZF + (16u << 10);
static constexpr size_t WS_HST = WS_CS1 + (8u << 10);

DEV f32x4 mfma32(bf16x8 a, bf16x8 b, f32x4 c) { return __builtin_amdgcn_mfma_f32_16x16x32_bf16(a, b, c, 0, 0, 0); }
DEV f32x4 mfma16(bf16x4 a, bf16x4 b, f32x4 c) { return __builtin_amdgcn_mfma_f32_16x16x16bf16_1k(a, b, c, 0, 0, 0); }

DEV unsigned pk2(float a, float b) {
  f32x2 v = {a, b};
  bfv2 r = __builtin_convertvector(v, bfv2);
  return __builtin_bit_cast(unsigned, r);
}
DEV float bflo(unsigned u) { return __builtin_bit_cast(float, u << 16); }
DEV float bfhi(unsigned u) { return __builtin_bit_cast(float, u & 0xffff0000u); }
DEV float bfr(float a) { return bflo(pk2(a, a)); }

DEV u32x4 ldg16(const void* p) { return *(const u32x4*)p; }
DEV u32x2 ldg8(const void* p) { return *(const u32x2*)p; }
DEV bf16x8 as_bf8(u32x4 v) { return __builtin_bit_cast(bf16x8, v); }
DEV bf16x4 as_bf4(u32x2 v) { return __builtin_bit_cast(bf16x4, v); }

DEV float phi_f(float t) {
  float e = __builtin_amdgcn_exp2f(t * 1.44269504088896341f);
  return (t > 0.f ? t + 1.f : e) + 1e-6f;
}

#define WAIT_V(n) asm volatile("s_waitcnt vmcnt(" #n ")" ::: "memory")
#define WAIT_L0() asm volatile("s_waitcnt lgkmcnt(0)" ::: "memory")
#define BAR() __builtin_amdgcn_s_barrier()

template <int N> DEV void wait_vm() { asm volatile("s_waitcnt vmcnt(%0)" ::"n"(N) : "memory"); }

DEV void ln_scalars(float mean, float var, float& a, float& b) {
  float mb = bfr(mean);
  float d = bfr(__builtin_sqrtf(bfr(bfr(var) + 1e-5f)));
  a = 1.0f / d;
  b = mb * a;
}

static constexpr int K1_W = 96 * 1024;
static constexpr int K1_ST = K1_W;
static constexpr int K1_CS = K1_ST + 1024;
static constexpr int K1_KV = K1_CS + 1536;
static constexpr int K1_Z = K1_KV + 8192;
static constexpr int K1_LDS = K1_Z + 512;

__global__ __launch_bounds__(256, 1) void k1_qkv(const char* __restrict__ x, const char* __restrict__ wqkv,
                                                  char* __restrict__ ws) {
  __shared__ __attribute__((aligned(16))) char lds[K1_LDS];
  const int tid = threadIdx.x, l = tid & 63, i = l & 15, g = l >> 4;
  const int w = __builtin_amdgcn_readfirstlane(tid >> 6);
  const int b = blockIdx.x, xcd = b & 7, idx = b >> 3;
  const int rt = xcd * 2 + (idx >> 4), p = idx & 15;

  const char* xp = x + (size_t)(rt * 128 + w * 32 + i) * 1024 + g * 16;
  const int c4 = (l & 3) ^ ((l >> 5) << 1);
  const char* wsrc = wqkv + (size_t)(l >> 2) * 3072 + p * 64 + c4 * 16;
  u32x4 xf[2][16];
#pragma unroll
  for (int st = 0; st < 8; ++st) {
#pragma unroll
    for (int u = 0; u < 3; ++u) {
      const int id = w * 3 + u;
      const int ks = 2 * st + id / 6, mu = (id % 6) >> 1, half = id & 1;
      __builtin_amdgcn_global_load_lds((const unsigned*)(wsrc + (size_t)(ks * 32 + half * 16) * 3072 + mu * 1024),
                                       (LDS_AS unsigned*)(lds + ks * 6144 + mu * 2048 + half * 1024), 16, 0, 0);
    }
#pragma unroll
    for (int k2 = 0; k2 < 2; ++k2) {
      xf[0][2 * st + k2] = ldg16(xp + (2 * st + k2) * 64);
      xf[1][2 * st + k2] = ldg16(xp + 16 * 1024 + (2 * st + k2) * 64);
    }
  }

  f32x4 acc[2][6];
  f32x4 ssum[2], ssq[2], cs[6];
#pragma unroll
  for (int m = 0; m < 2; ++m) {
#pragma unroll
    for (int n = 0; n < 6; ++n) acc[m][n] = (f32x4){0.f, 0.f, 0.f, 0.f};
    ssum[m] = (f32x4){0.f, 0.f, 0.f, 0.f};
    ssq[m] = (f32x4){0.f, 0.f, 0.f, 0.f};
  }
#pragma unroll
  for (int n = 0; n < 6; ++n) cs[n] = (f32x4){0.f, 0.f, 0.f, 0.f};
  const u32x4 onesu = {0x3f803f80u, 0x3f803f80u, 0x3f803f80u, 0x3f803f80u};
  const bf16x8 ones = as_bf8(onesu);

  const int q4 = i >> 2, pp = i & 3;
  const int trb0 = (8 * g + q4) * 64 + 16 * ((pp >> 1) ^ (2 * (g & 1))) + 8 * (pp & 1);
  const int trb1 = trb0 ^ 32;

#pragma unroll
  for (int st = 0; st < 8; ++st) {
    if (st == 0) wait_vm<49>(); else if (st == 1) wait_vm<42>(); else if (st == 2) wait_vm<35>();
    else if (st == 3) wait_vm<28>(); else if (st == 4) wait_vm<21>(); else if (st == 5) wait_vm<14>();
    else if (st == 6) wait_vm<7>(); else wait_vm<0>();
    BAR();
#pragma unroll
    for (int k2 = 0; k2 < 2; ++k2) {
      const int ks = 2 * st + k2;
      bf16x8 wf[6];
#pragma unroll
      for (int n = 0; n < 6; ++n) {
        const int off = ks * 6144 + (n >> 1) * 2048 + ((n & 1) ? trb1 : trb0);
        v4i16_t lo = __builtin_amdgcn_ds_read_tr16_b64_v4i16((LDS_AS v4i16_t*)(lds + off));
        v4i16_t hi = __builtin_amdgcn_ds_read_tr16_b64_v4i16((LDS_AS v4i16_t*)(lds + off + 256));
        wf[n] = __builtin_shufflevector(lo, hi, 0, 1, 2, 3, 4, 5, 6, 7);
      }
#pragma unroll
      for (int m = 0; m < 2; ++m) {
        const bf16x8 xv = as_bf8(xf[m][ks]);
        acc[m][0] = mfma32(wf[0], xv, acc[m][0]);
        acc[m][1] = mfma32(wf[1], xv, acc[m][1]);
#pragma unroll
        for (int n = 2; n < 6; ++n) acc[m][n] = mfma32(xv, wf[n], acc[m][n]);
        ssum[m] = mfma32(ones, xv, ssum[m]);
        ssq[m] = mfma32(xv, xv, ssq[m]);
      }
      if ((ks & 3) == w) {
        cs[0] = mfma32(wf[0], ones, cs[0]);
        cs[1] = mfma32(wf[1], ones, cs[1]);
#pragma unroll
        for (int n = 2; n < 6; ++n) cs[n] = mfma32(ones, wf[n], cs[n]);
      }
    }
  }

#pragma unroll
  for (int m = 0; m < 2; ++m) {
    const int r = i & 3;
    float sq = r == 0 ? ssq[m][0] : (r == 1 ? ssq[m][1] : (r == 2 ? ssq[m][2] : ssq[m][3]));
    float mean = ssum[m][0] * (1.0f / 512.0f);
    float var = fmaxf(sq * (1.0f / 512.0f) - mean * mean, 0.f);
    float a, bb;
    ln_scalars(mean, var, a, bb);
    if (g == (i >> 2)) *(f32x2*)(lds + K1_ST + w * 256 + m * 128 + i * 8) = (f32x2){a, bb};
  }
  if (i == 0) {
    *(f32x4*)(lds + K1_CS + w * 384 + 0 * 64 + g * 16) = cs[0];
    *(f32x4*)(lds + K1_CS + w * 384 + 1 * 64 + g * 16) = cs[1];
  }
  if (g == 0) {
#pragma unroll
    for (int n = 2; n < 6; ++n) *(float*)(lds + K1_CS + w * 384 + n * 64 + i * 4) = cs[n][0];
  }
  __syncthreads();
  f32x4 csq[2];
  float csn[4];
#pragma unroll
  for (int n = 0; n < 2; ++n) {
    csq[n] = *(const f32x4*)(lds + K1_CS + n * 64 + g * 16);
#pragma unroll
    for (int ww = 1; ww < 4; ++ww) csq[n] += *(const f32x4*)(lds + K1_CS + ww * 384 + n * 64 + g * 16);
  }
#pragma unroll
  for (int n = 0; n < 4; ++n) {
    csn[n] = *(const float*)(lds + K1_CS + (n + 2) * 64 + i * 4);
#pragma unroll
    for (int ww = 1; ww < 4; ++ww) csn[n] += *(const float*)(lds + K1_CS + ww * 384 + (n + 2) * 64 + i * 4);
  }

#pragma unroll
  for (int m = 0; m < 2; ++m) {
    const f32x2 ab = *(const f32x2*)(lds + K1_ST + w * 256 + m * 128 + i * 8);
    u32x4 o;
#pragma unroll
    for (int n = 0; n < 2; ++n) {
      float v[4];
#pragma unroll
      for (int r = 0; r < 4; ++r) v[r] = phi_f(bfr(acc[m][n][r] * ab[0] - ab[1] * csq[n][r]));
      o[2 * n] = pk2(v[0], v[1]);
      o[2 * n + 1] = pk2(v[2], v[3]);
    }
    const int mt = rt * 8 + w * 2 + m;
    *(u32x4*)(ws + WS_PQ + (size_t)(mt * 16 + p) * 1024 + l * 16) = o;
  }
  {
    u32x4 pkf[2], vf[2];
#pragma unroll
    for (int m = 0; m < 2; ++m) {
      const f32x4 s0 = *(const f32x4*)(lds + K1_ST + w * 256 + m * 128 + g * 32);
      const f32x4 s1 = *(const f32x4*)(lds + K1_ST + w * 256 + m * 128 + g * 32 + 16);
      const float av[4] = {s0[0], s0[2], s1[0], s1[2]};
      const float bv[4] = {s0[1], s0[3], s1[1], s1[3]};
#pragma unroll
      for (int hh = 0; hh < 2; ++hh) {
        float kk[4], vv[4];
#pragma unroll
        for (int r = 0; r < 4; ++r) {
          kk[r] = phi_f(bfr(acc[m][2 + hh][r] * av[r] - bv[r] * csn[hh]));
          vv[r] = acc[m][4 + hh][r] * av[r] - bv[r] * csn[2 + hh];
        }
        pkf[hh][2 * m] = pk2(kk[0], kk[1]);
        pkf[hh][2 * m + 1] = pk2(kk[2], kk[3]);
        vf[hh][2 * m] = pk2(vv[0], vv[1]);
        vf[hh][2 * m + 1] = pk2(vv[2], vv[3]);
      }
    }
#pragma unroll
    for (int hh = 0; hh < 2; ++hh) {
      const f32x4 z4 = {0.f, 0.f, 0.f, 0.f};
      f32x4 kvt = mfma32(as_bf8(pkf[hh]), as_bf8(vf[hh]), z4);
      f32x4 zz = mfma32(ones, as_bf8(pkf[hh]), z4);
      *(f32x4*)(lds + K1_KV + w * 2048 + hh * 1024 + l * 16) = kvt;
      if (g == 0) *(float*)(lds + K1_Z + w * 128 + hh * 64 + i * 4) = zz[0];
    }
  }
  __syncthreads();
  if (tid < 128) {
    f32x4 s = *(const f32x4*)(lds + K1_KV + tid * 16);
#pragma unroll
    for (int ww = 1; ww < 4; ++ww) s += *(const f32x4*)(lds + K1_KV + ww * 2048 + tid * 16);
    *(f32x4*)(ws + WS_KVP + (size_t)(rt * 32 + 2 * p) * 1024 + tid * 16) = s;
  } else if (tid < 160) {
    const int t = tid - 128;
    float s = *(const float*)(lds + K1_Z + t * 4);
#pragma unroll
    for (int ww = 1; ww < 4; ++ww) s += *(const float*)(lds + K1_Z + ww * 128 + t * 4);
    *(float*)(ws + WS_ZP + (size_t)(rt * 32 + 2 * p) * 64 + t * 4) = s;
  }
}

DEV unsigned perm_lo(unsigned a, unsigned b) { return __builtin_amdgcn_perm(b, a, 0x05040100u); }
DEV unsigned perm_hi(unsigned a, unsigned b) { return __builtin_amdgcn_perm(b, a, 0x07060302u); }

__global__ __launch_bounds__(256) void kr_prep(const char* __restrict__ wo, const char* __restrict__ w1,
                                               const char* __restrict__ w2, char* __restrict__ ws) {
  __shared__ float red[16][64];
  const int tid = threadIdx.x, l = tid & 63, i = l & 15, g = l >> 4, w = tid >> 6;
  const int b = blockIdx.x;
  if (b < 8) {
    const int h = b * 4 + w;
    f32x4 s = {0.f, 0.f, 0.f, 0.f}, z = {0.f, 0.f, 0.f, 0.f};
    f32x4 t[16], tz[16];
#pragma unroll
    for (int r = 0; r < 16; ++r) {
      t[r] = *(const f32x4*)(ws + WS_KVP + (size_t)(r * 32 + h) * 1024 + l * 16);
      tz[r] = *(const f32x4*)(ws + WS_ZP + (size_t)(r * 32 + h) * 64 + g * 16);
    }
#pragma unroll
    for (int r = 0; r < 16; ++r) { s += t[r]; z += tz[r]; }
    *(u32x2*)(ws + WS_KVF + (h >> 1) * 1024 + l * 16 + (h & 1) * 8) = (u32x2){pk2(s[0], s[1]), pk2(s[2], s[3])};
    *(u32x2*)(ws + WS_ZF + (h >> 1) * 1024 + l * 16 + (h & 1) * 8) = (u32x2){pk2(z[0], z[1]), pk2(z[2], z[3])};
  } else if (b < 264) {
    const int u = (b - 8) * 4 + w;
    const char* src;
    char* dst;
    size_t ldb;
    size_t tstride;
    int kb[2];
    int kr;
    if (u < 128) {
      const int q = u >> 4, s = u & 15;
      src = wo + q * 128 + i * 8; ldb = 1024;
      kb[0] = 32 * s + 4 * g; kb[1] = kb[0] + 16; kr = 1;
      dst = ws + WS_WO + (size_t)(q * 16 + s) * 4096; tstride = 1024;
    } else if (u < 576) {
      const int v = u - 128, q = v >> 4, s = v & 15;
      src = w1 + q * 128 + i * 8; ldb = 3584;
      kb[0] = 64 * (s >> 1) + 16 * g + 2 * (s & 1); kb[1] = kb[0] + 1; kr = 4;
      dst = ws + WS_W1 + (size_t)(s * 112 + q * 4) * 1024; tstride = 1024;
    } else {
      const int v = u - 576, q = v / 56, s = v % 56;
      src = w2 + q * 128 + i * 8; ldb = 1024;
      kb[0] = 64 * (s >> 1) + 16 * g + 2 * (s & 1); kb[1] = kb[0] + 1; kr = 4;
      dst = ws + WS_W2 + (size_t)(q * 56 + s) * 4096; tstride = 1024;
    }
    u32x2 v8[8];
#pragma unroll
    for (int j = 0; j < 8; ++j) v8[j] = ldg8(src + (size_t)(kb[j >> 2] + (j & 3) * kr) * ldb);
#pragma unroll
    for (int e = 0; e < 4; ++e) {
      u32x4 o;
#pragma unroll
      for (int jj = 0; jj < 4; ++jj) {
        const unsigned a = v8[2 * jj][e >> 1], c = v8[2 * jj + 1][e >> 1];
        o[jj] = (e & 1) ? perm_hi(a, c) : perm_lo(a, c);
      }
      *(u32x4*)(dst + e * tstride + l * 16) = o;
    }
  } else {
    const int q = b - 264;
    const int rho = tid >> 4, c = tid & 15;
    const char* src = w1 + q * 128 + c * 8 + (size_t)rho * 3584;
    float s0 = 0.f, s1 = 0.f, s2 = 0.f, s3 = 0.f;
    u32x2 t[32];
#pragma unroll
    for (int r = 0; r < 32; ++r) t[r] = ldg8(src + (size_t)r * 16 * 3584);
#pragma unroll
    for (int r = 0; r < 32; ++r) {
      s0 += bflo(t[r][0]); s1 += bfhi(t[r][0]); s2 += bflo(t[r][1]); s3 += bfhi(t[r][1]);
    }
    red[rho][c * 4 + 0] = s0; red[rho][c * 4 + 1] = s1; red[rho][c * 4 + 2] = s2; red[rho][c * 4 + 3] = s3;
    __syncthreads();
    if (tid < 64) {
      float s = 0.f;
#pragma unroll
      for (int r = 0; r < 16; ++r) s += red[r][tid];
      *(float*)(ws + WS_CS1 + (q * 64 + tid) * 4) = s;
    }
  }
}

__global__ __launch_bounds__(256, 1) void k2_attn(const char* __restrict__ x, char* __restrict__ ws) {
  __shared__ __attribute__((aligned(16))) char lds[4 * 3 * 4096];
  const int tid = threadIdx.x, l = tid & 63, i = l & 15, g = l >> 4;
  const int w = __builtin_amdgcn_readfirstlane(tid >> 6);
  const int b = blockIdx.x, xcd = b & 7, idx = b >> 3;
  const int rt = xcd * 4 + (idx >> 3), q = idx & 7;

  u32x4 kvf[4], zf[4], pqf[4][4], wf[4][4];
#pragma unroll
  for (int s = 0; s < 4; ++s) {
    const int sg = 4 * w + s;
    kvf[s] = ldg16(ws + WS_KVF + sg * 1024 + l * 16);
    zf[s] = ldg16(ws + WS_ZF + sg * 1024 + l * 16);
#pragma unroll
    for (int m = 0; m < 4; ++m) pqf[s][m] = ldg16(ws + WS_PQ + (size_t)((rt * 4 + m) * 16 + sg) * 1024 + l * 16);
#pragma unroll
    for (int e = 0; e < 4; ++e) wf[s][e] = ldg16(ws + WS_WO + (size_t)((q * 16 + sg) * 4 + e) * 1024 + l * 16);
  }
  const int row = rt * 64 + w * 16 + i;
  const u32x4 xr0 = ldg16(x + (size_t)row * 1024 + q * 128 + g * 32);
  const u32x4 xr1 = ldg16(x + (size_t)row * 1024 + q * 128 + g * 32 + 16);

  f32x4 acc[4][4];
#pragma unroll
  for (int m = 0; m < 4; ++m)
#pragma unroll
    for (int e = 0; e < 4; ++e) acc[m][e] = (f32x4){0.f, 0.f, 0.f, 0.f};
  const f32x4 z4 = {0.f, 0.f, 0.f, 0.f};
#pragma unroll
  for (int s = 0; s < 4; ++s) {
#pragma unroll
    for (int m = 0; m < 4; ++m) {
      u32x4 of;
#pragma unroll
      for (int hh = 0; hh < 2; ++hh) {
        const bf16x4 pq = as_bf4((u32x2){pqf[s][m][2 * hh], pqf[s][m][2 * hh + 1]});
        const f32x4 num = mfma16(as_bf4((u32x2){kvf[s][2 * hh], kvf[s][2 * hh + 1]}), pq, z4);
        const f32x4 den = mfma16(as_bf4((u32x2){zf[s][2 * hh], zf[s][2 * hh + 1]}), pq, z4);
        const float rd = __builtin_amdgcn_rcpf(bfr(bfr(den[0]) + 1e-6f));
        of[2 * hh] = pk2(num[0] * rd, num[1] * rd);
        of[2 * hh + 1] = pk2(num[2] * rd, num[3] * rd);
      }
#pragma unroll
      for (int e = 0; e < 4; ++e) acc[m][e] = mfma32(as_bf8(wf[s][e]), as_bf8(of), acc[m][e]);
    }
  }
#pragma unroll
  for (int m = 0; m < 4; ++m) {
    if (m != w) {
      const int slot = (w - m - 1) & 3;
#pragma unroll
      for (int e = 0; e < 4; ++e) *(f32x4*)(lds + (m * 3 + slot) * 4096 + e * 1024 + l * 16) = acc[m][e];
    }
  }
  __syncthreads();
  f32x4 fin[4];
#pragma unroll
  for (int e = 0; e < 4; ++e) fin[e] = w == 0 ? acc[0][e] : (w == 1 ? acc[1][e] : (w == 2 ? acc[2][e] : acc[3][e]));
#pragma unroll
  for (int sl = 0; sl < 3; ++sl)
#pragma unroll
    for (int e = 0; e < 4; ++e) fin[e] += *(const f32x4*)(lds + (w * 3 + sl) * 4096 + e * 1024 + l * 16);

  float hv[4][4];
  float sum = 0.f;
#pragma unroll
  for (int r = 0; r < 4; ++r) {
    const unsigned lo = r < 2 ? xr0[2 * r] : xr1[2 * r - 4];
    const unsigned hi = r < 2 ? xr0[2 * r + 1] : xr1[2 * r - 3];
    hv[0][r] = bfr(bflo(lo) + bfr(fin[0][r]));
    hv[1][r] = bfr(bfhi(lo) + bfr(fin[1][r]));
    hv[2][r] = bfr(bflo(hi) + bfr(fin[2][r]));
    hv[3][r] = bfr(bfhi(hi) + bfr(fin[3][r]));
    sum += (hv[0][r] + hv[1][r]) + (hv[2][r] + hv[3][r]);
  }
  const int mt = rt * 4 + w;
#pragma unroll
  for (int t = 0; t < 2; ++t) {
    u32x4 o = {pk2(hv[2 * t][0], hv[2 * t][1]), pk2(hv[2 * t][2], hv[2 * t][3]),
               pk2(hv[2 * t + 1][0], hv[2 * t + 1][1]), pk2(hv[2 * t + 1][2], hv[2 * t + 1][3])};
    *(u32x4*)(ws + WS_H + (size_t)(mt * 16 + 2 * q + t) * 1024 + l * 16) = o;
  }
  sum += __shfl_xor(sum, 16);
  sum += __shfl_xor(sum, 32);
  const float gm = sum * (1.0f / 64.0f);
  float m2 = 0.f;
#pragma unroll
  for (int e = 0; e < 4; ++e)
#pragma unroll
    for (int r = 0; r < 4; ++r) { const float d = hv[e][r] - gm; m2 += d * d; }
  m2 += __shfl_xor(m2, 16);
  m2 += __shfl_xor(m2, 32);
  if (g == 0) *(f32x2*)(ws + WS_HST + (size_t)row * 64 + q * 8) = (f32x2){gm, m2};
}

__global__ __launch_bounds__(256, 1) void k3_up(const char* __restrict__ b1, char* __restrict__ ws) {
  __shared__ __attribute__((aligned(16))) char lds[112 * 1024];
  const int tid = threadIdx.x, l = tid & 63, i = l & 15, g = l >> 4;
  const int w = __builtin_amdgcn_readfirstlane(tid >> 6);
  const int b = blockIdx.x, xcd = b & 7, idx = b >> 3;
  const int rt = xcd * 2 + (idx >> 4), cb = idx & 15;
  const int mt0 = rt * 8 + w * 2;

  u32x4 xf[2][16];
  const char* wsrc = ws + WS_W1 + (size_t)(cb * 7) * 1024 + l * 16;
  const char* xsrc = ws + WS_H + (size_t)(mt0 * 16) * 1024 + l * 16;
#pragma unroll
  for (int st = 0; st < 4; ++st) {
    const int ks = 4 * st + w;
#pragma unroll
    for (int n = 0; n < 7; ++n)
      __builtin_amdgcn_global_load_lds((const unsigned*)(wsrc + (size_t)(ks * 112 + n) * 1024),
                                       (LDS_AS unsigned*)(lds + (ks * 7 + n) * 1024), 16, 0, 0);
#pragma unroll
    for (int k2 = 0; k2 < 4; ++k2) {
      xf[0][4 * st + k2] = ldg16(xsrc + (4 * st + k2) * 1024);
      xf[1][4 * st + k2] = ldg16(xsrc + (16 + 4 * st + k2) * 1024);
    }
  }
  f32x4 stp[2];
#pragma unroll
  for (int m = 0; m < 2; ++m) stp[m] = *(const f32x4*)(ws + WS_HST + (size_t)((mt0 + m) * 16 + i) * 64 + g * 16);

  f32x4 acc[2][7];
#pragma unroll
  for (int m = 0; m < 2; ++m)
#pragma unroll
    for (int n = 0; n < 7; ++n) acc[m][n] = (f32x4){0.f, 0.f, 0.f, 0.f};

#pragma unroll
  for (int st = 0; st < 4; ++st) {
    if (st == 0) wait_vm<47>(); else if (st == 1) wait_vm<32>(); else if (st == 2) wait_vm<17>(); else wait_vm<2>();
    BAR();
#pragma unroll
    for (int k2 = 0; k2 < 4; ++k2) {
      const int ks = 4 * st + k2;
#pragma unroll
      for (int n = 0; n < 7; ++n) {
        const bf16x8 wv = *(const bf16x8*)(lds + (ks * 7 + n) * 1024 + l * 16);
        acc[0][n] = mfma32(wv, as_bf8(xf[0][ks]), acc[0][n]);
        acc[1][n] = mfma32(wv, as_bf8(xf[1][ks]), acc[1][n]);
      }
    }
  }

  float la[2], lb[2];
#pragma unroll
  for (int m = 0; m < 2; ++m) {
    float s = stp[m][0] + stp[m][2];
    s += __shfl_xor(s, 16);
    s += __shfl_xor(s, 32);
    const float mean = s * 0.125f;
    const float d0 = stp[m][0] - mean, d1 = stp[m][2] - mean;
    float m2 = stp[m][1] + stp[m][3] + 64.f * (d0 * d0 + d1 * d1);
    m2 += __shfl_xor(m2, 16);
    m2 += __shfl_xor(m2, 32);
    const float var = m2 * (1.0f / 512.0f);
    const float mb = bfr(mean);
    const float r1 = 1.0f / bfr(__builtin_sqrtf(bfr(bfr(var) + 1e-5f)));
    const float mn2 = bfr((mean - mb) * r1);
    const float var2 = var * r1 * r1;
    const float r2 = 1.0f / bfr(__builtin_sqrtf(bfr(bfr(var2) + 1e-5f)));
    la[m] = r1 * r2;
    lb[m] = (mb * r1 + mn2) * r2;
  }
#pragma unroll
  for (int n = 0; n < 7; ++n) {
    const int f = cb * 7 + n;
    const int col0 = 64 * (f >> 2) + 16 * g + (f & 3);
    float csv[4], bv[4];
#pragma unroll
    for (int r = 0; r < 4; ++r) {
      csv[r] = *(const float*)(ws + WS_CS1 + (col0 + 4 * r) * 4);
      bv[r] = bflo(*(const unsigned short*)(b1 + (col0 + 4 * r) * 2));
    }
#pragma unroll
    for (int m = 0; m < 2; ++m) {
      float gl[4];
#pragma unroll
      for (int r = 0; r < 4; ++r) {
        const float t = acc[m][n][r] * la[m] + (bv[r] - lb[m] * csv[r]);
        const float u = t * (t * t * (-0.044715f * 2.302208198f) - 2.302208198f);
        gl[r] = t * __builtin_amdgcn_rcpf(1.0f + __builtin_amdgcn_exp2f(u));
      }
      *(u32x2*)(ws + WS_HM + (size_t)((mt0 + m) * 56 + (f >> 1)) * 1024 + l * 16 + (f & 1) * 8) =
          (u32x2){pk2(gl[0], gl[1]), pk2(gl[2], gl[3])};
    }
  }
}

__global__ __launch_bounds__(256, 1) void k4_down(const char* __restrict__ b2, const char* __restrict__ ws,
                                                   char* __restrict__ out) {
  __shared__ __attribute__((aligned(16))) char lds[4 * 3 * 4096];
  const int tid = threadIdx.x, l = tid & 63, i = l & 15, g = l >> 4;
  const int w = __builtin_amdgcn_readfirstlane(tid >> 6);
  const int b = blockIdx.x, xcd = b & 7, idx = b >> 3;
  const int rt = xcd * 4 + (idx >> 3), q = idx & 7;

  const char* xsrc = ws + WS_HM + (size_t)((rt * 4) * 56 + 14 * w) * 1024 + l * 16;
  const char* wsrc = ws + WS_W2 + (size_t)((q * 56 + 14 * w) * 4) * 1024 + l * 16;
  u32x4 xf[7][4], wf[7][4];
#pragma unroll
  for (int s = 0; s < 7; ++s) {
#pragma unroll
    for (int m = 0; m < 4; ++m) xf[s][m] = ldg16(xsrc + (size_t)(m * 56 + s) * 1024);
#pragma unroll
    for (int e = 0; e < 4; ++e) wf[s][e] = ldg16(wsrc + (size_t)(s * 4 + e) * 1024);
  }
  f32x4 acc[4][4];
#pragma unroll
  for (int m = 0; m < 4; ++m)
#pragma unroll
    for (int e = 0; e < 4; ++e) acc[m][e] = (f32x4){0.f, 0.f, 0.f, 0.f};
#pragma unroll
  for (int s = 0; s < 7; ++s) {
#pragma unroll
    for (int m = 0; m < 4; ++m)
#pragma unroll
      for (int e = 0; e < 4; ++e) acc[m][e] = mfma32(as_bf8(wf[s][e]), as_bf8(xf[s][m]), acc[m][e]);
    __builtin_amdgcn_sched_barrier(0);
#pragma unroll
    for (int m = 0; m < 4; ++m) xf[s][m] = ldg16(xsrc + (size_t)(m * 56 + 7 + s) * 1024);
#pragma unroll
    for (int e = 0; e < 4; ++e) wf[s][e] = ldg16(wsrc + (size_t)((7 + s) * 4 + e) * 1024);
    __builtin_amdgcn_sched_barrier(0);
  }
  const int mt = rt * 4 + w;
  const u32x4 h0 = ldg16(ws + WS_H + (size_t)(mt * 16 + 2 * q) * 1024 + l * 16);
  const u32x4 h1 = ldg16(ws + WS_H + (size_t)(mt * 16 + 2 * q + 1) * 1024 + l * 16);
  const u32x4 bb0 = ldg16(b2 + q * 128 + g * 32);
  const u32x4 bb1 = ldg16(b2 + q * 128 + g * 32 + 16);
#pragma unroll
  for (int s = 0; s < 7; ++s) {
#pragma unroll
    for (int m = 0; m < 4; ++m)
#pragma unroll
      for (int e = 0; e < 4; ++e) acc[m][e] = mfma32(as_bf8(wf[s][e]), as_bf8(xf[s][m]), acc[m][e]);
  }
#pragma unroll
  for (int m = 0; m < 4; ++m) {
    if (m != w) {
      const int slot = (w - m - 1) & 3;
#pragma unroll
      for (int e = 0; e < 4; ++e) *(f32x4*)(lds + (m * 3 + slot) * 4096 + e * 1024 + l * 16) = acc[m][e];
    }
  }
  __syncthreads();
  f32x4 fin[4];
#pragma unroll
  for (int e = 0; e < 4; ++e) fin[e] = w == 0 ? acc[0][e] : (w == 1 ? acc[1][e] : (w == 2 ? acc[2][e] : acc[3][e]));
#pragma unroll
  for (int sl = 0; sl < 3; ++sl)
#pragma unroll
    for (int e = 0; e < 4; ++e) fin[e] += *(const f32x4*)(lds + (w * 3 + sl) * 4096 + e * 1024 + l * 16);

  float ov[4][4];
#pragma unroll
  for (int e = 0; e < 4; ++e) {
#pragma unroll
    for (int r = 0; r < 4; ++r) {
      const unsigned hu = (e < 2 ? h0 : h1)[2 * (e & 1) + (r >> 1)];
      const float hval = (r & 1) ? bfhi(hu) : bflo(hu);
      const unsigned bu = (r < 2 ? bb0 : bb1)[2 * (r & 1) + (e >> 1)];
      const float bval = (e & 1) ? bfhi(bu) : bflo(bu);
      ov[e][r] = hval + bfr(bfr(fin[e][r]) + bval);
    }
  }
  const int row = rt * 64 + w * 16 + i;
  u32x4 o0 = {pk2(ov[0][0], ov[1][0]), pk2(ov[2][0], ov[3][0]), pk2(ov[0][1], ov[1][1]), pk2(ov[2][1], ov[3][1])};
  u32x4 o1 = {pk2(ov[0][2], ov[1][2]), pk2(ov[2][2], ov[3][2]), pk2(ov[0][3], ov[1][3]), pk2(ov[2][3], ov[3][3])};
  *(u32x4*)(out + (size_t)row * 1024 + q * 128 + g * 32) = o0;
  *(u32x4*)(out + (size_t)row * 1024 + q * 128 + g * 32 + 16) = o1;
}

extern "C" void kernel_launch(void* const* d_in, const int* in_sizes, int n_in, void* d_out, int out_size,
                              void* d_ws, size_t ws_size, hipStream_t stream) {
  const char* x = (const char*)d_in[0];
  const char* wqkv = (const char*)d_in[1];
  const char* wo = (const char*)d_in[2];
  const char* w1 = (const char*)d_in[4];
  const char* b1 = (const char*)d_in[5];
  const char* w2 = (const char*)d_in[6];
  const char* b2 = (const char*)d_in[7];
  char* ws = (char*)d_ws;
  k1_qkv<<<256, 256, 0, stream>>>(x, wqkv, ws);
  kr_prep<<<292, 256, 0, stream>>>(wo, w1, w2, ws);
  k2_attn<<<256, 256, 0, stream>>>(x, ws);
  k3_up<<<256, 256, 0, stream>>>(b1, ws);
  k4_down<<<256, 256, 0, stream>>>(b2, ws, (char*)d_out);
}
```

```cpp
#include <hip/hip_runtime.h>
#include <hip/hip_bf16.h>
#include <stdint.h>

typedef __attribute__((ext_vector_type(8))) short bf16x8;
typedef __attribute__((ext_vector_type(4))) short bf16x4;
typedef __attribute__((ext_vector_type(4))) short v4i16_t;
typedef __attribute__((ext_vector_type(4))) float f32x4;
typedef __attribute__((ext_vector_type(2))) float f32x2;
typedef __attribute__((ext_vector_type(4))) unsigned u32x4;
typedef __attribute__((ext_vector_type(2))) unsigned u32x2;
typedef __attribute__((ext_vector_type(2))) __bf16 bfv2;

#define DEV __device__ __forceinline__
#define LDS_AS __attribute__((address_space(3)))

static constexpr size_t WS_PQ  = 0;
static constexpr size_t WS_H   = WS_PQ + (2u << 20);
static constexpr size_t WS_HM  = WS_H + (2u << 20);
static constexpr size_t WS_WO  = WS_HM + (7u << 20);
static constexpr size_t WS_W1  = WS_WO + (512u << 10);
static constexpr size_t WS_W2  = WS_W1 + (1792u << 10);
static constexpr size_t WS_KVP = WS_W2 + (1792u << 10);
static constexpr size_t WS_ZP  = WS_KVP + (512u << 10);
static constexpr size_t WS_KVF = WS_ZP + (32u << 10);
static constexpr size_t WS_ZF  = WS_KVF + (16u << 10);
static constexpr size_t WS_CS1 = WS_ZF + (16u << 10);
static constexpr size_t WS_HST = WS_CS1 + (24u << 10);

DEV f32x4 mfma32(bf16x8 a, bf16x8 b, f32x4 c) { return __builtin_amdgcn_mfma_f32_16x16x32_bf16(a, b, c, 0, 0, 0); }
DEV f32x4 mfma16(bf16x4 a, bf16x4 b, f32x4 c) { return __builtin_amdgcn_mfma_f32_16x16x16bf16_1k(a, b, c, 0, 0, 0); }

DEV unsigned pk2(float a, float b) {
  f32x2 v = {a, b};
  bfv2 r = __builtin_convertvector(v, bfv2);
  return __builtin_bit_cast(unsigned, r);
}
DEV float bflo(unsigned u) { return __builtin_bit_cast(float, u << 16); }
DEV float bfhi(unsigned u) { return __builtin_bit_cast(float, u & 0xffff0000u); }
DEV float bfr(float a) { return bflo(pk2(a, a)); }

DEV u32x4 ldg16(const void* p) { return *(const u32x4*)p; }
DEV u32x2 ldg8(const void* p) { return *(const u32x2*)p; }
DEV bf16x8 as_bf8(u32x4 v) { return __builtin_bit_cast(bf16x8, v); }
DEV bf16x4 as_bf4(u32x2 v) { return __builtin_bit_cast(bf16x4, v); }

DEV float phi_f(float t) {
  float e = __builtin_amdgcn_exp2f(t * 1.44269504088896341f);
  return (t > 0.f ? t + 1.f : e) + 1e-6f;
}

#define WAIT_V(n) asm volatile("s_waitcnt vmcnt(" #n ")" ::: "memory")
#define WAIT_L0() asm volatile("s_waitcnt lgkmcnt(0)" ::: "memory")
#define BAR() __builtin_amdgcn_s_barrier()

#define TS_DECL
#define TS(k)
#define TS_END(kid, n, wsp)
template <int N> DEV void wait_vm() { asm volatile("s_waitcnt vmcnt(%0)" ::"n"(N) : "memory"); }

DEV void ln_scalars(float mean, float var, float& a, float& b) {
  float mb = bfr(mean);
  float d = bfr(__builtin_sqrtf(bfr(bfr(var) + 1e-5f)));
  a = 1.0f / d;
  b = mb * a;
}

static constexpr int K1_CSO = 131072;
static constexpr int K1_LDS = K1_CSO + 2048;

DEV void glds16(unsigned voff, const char* sbase, unsigned ldsdst) {
  unsigned keep;
  asm volatile("s_mov_b32 %0, m0\n\ts_mov_b32 m0, %3\n\ts_nop 0\n\tglobal_load_lds_dwordx4 %1, %2\n\ts_mov_b32 m0, %0"
               : "=&s"(keep) : "v"(voff), "s"(sbase), "s"(ldsdst) : "memory");
}
DEV void gld16(u32x4& d, unsigned voff, const char* sbase) {
  asm volatile("global_load_dwordx4 %0, %1, %2" : "=v"(d) : "v"(voff), "s"(sbase) : "memory");
}
DEV void gld8(u32x2& d, unsigned voff, const char* sbase) {
  asm volatile("global_load_dwordx2 %0, %1, %2" : "=v"(d) : "v"(voff), "s"(sbase) : "memory");
}
template <int N> DEV void wait_vm2(u32x4& a, u32x4& b) {
  asm volatile("s_waitcnt vmcnt(%2)" : "+v"(a), "+v"(b) : "n"(N) : "memory");
}
DEV bf16x8 tr_frag(const char* lds, int off) {
  v4i16_t lo = __builtin_amdgcn_ds_read_tr16_b64_v4i16((LDS_AS v4i16_t*)(lds + off));
  v4i16_t hi = __builtin_amdgcn_ds_read_tr16_b64_v4i16((LDS_AS v4i16_t*)(lds + off + 512));
  return __builtin_shufflevector(lo, hi, 0, 1, 2, 3, 4, 5, 6, 7);
}
DEV void x_fix(const u32x4& a0, const u32x4& a1, bf16x8& r0, bf16x8& r1) {
  u32x4 p, q;
#pragma unroll
  for (int c = 0; c < 4; ++c) {
    p[c] = __builtin_amdgcn_update_dpp(a0[c], a1[c], 0x118, 0xf, 0xC, false);
    q[c] = __builtin_amdgcn_update_dpp(a1[c], a0[c], 0x108, 0xf, 0x3, false);
  }
  r0 = as_bf8(p);
  r1 = as_bf8(q);
}
DEV unsigned perm_lo(unsigned a, unsigned b) { return __builtin_amdgcn_perm(b, a, 0x05040100u); }
DEV unsigned perm_hi(unsigned a, unsigned b) { return __builtin_amdgcn_perm(b, a, 0x07060302u); }
DEV float bperm(int lane, float v) {
  return __builtin_bit_cast(float, __builtin_amdgcn_ds_bpermute(lane * 4, __builtin_bit_cast(int, v)));
}

__global__ __launch_bounds__(512, 2) void k1_qkv(const char* __restrict__ x, const char* __restrict__ wqkv,
                                                  const char* __restrict__ wo, const char* __restrict__ w1,
                                                  const char* __restrict__ w2, const char* __restrict__ b1,
                                                  char* __restrict__ ws) {
  __shared__ __attribute__((aligned(16))) char lds[K1_LDS];
  TS_DECL
  const int tid = threadIdx.x, l = tid & 63, i = l & 15, g = l >> 4;
  const int w = __builtin_amdgcn_readfirstlane(tid >> 6);
  const int b = blockIdx.x, xcd = b & 7, idx = b >> 3;
  const int rt = xcd * 2 + (idx >> 4), j = idx & 15;
  const int rot = (j + 4 * (idx >> 4) + xcd) & 7;
  const unsigned lds0 = (unsigned)(uintptr_t)(LDS_AS char*)lds;

  const unsigned xoff = (i & 7) * 1024 + (g + 4 * (i >> 3)) * 16;
  const char* xb = x + (size_t)(rt * 128 + w * 16) * 1024;
  const int gkey = ((l >> 4) & 1) + 2 * (w & 1);
  const unsigned woff = (l >> 3) * 3072 + (((((l & 7) >> 1) ^ gkey) << 1) + (l & 1)) * 16;
  const int q4 = i >> 2, pp = i & 3;
  const int trb = (8 * g + q4) * 128 + (((q4 >> 1) & 1) + 2 * (g & 1)) * 32 + pp * 8;
  const u32x4 onesu = {0x3f803f80u, 0x3f803f80u, 0x3f803f80u, 0x3f803f80u};
  const bf16x8 ones = as_bf8(onesu);
  const f32x4 z4 = {0.f, 0.f, 0.f, 0.f};
  u32x4 xa[8][2];

  if (j < 8) {
    const char* wb = wqkv + (size_t)(w * 8) * 3072 + 1024 + j * 128;
#pragma unroll
    for (int st = 0; st < 8; ++st) {
      const int sr = (st + rot) & 7;
      glds16(woff, wb + (size_t)sr * (64 * 3072), lds0 + st * 16384 + w * 1024);
      glds16(woff, wb + (size_t)sr * (64 * 3072) + 1024, lds0 + st * 16384 + 8192 + w * 1024);
      gld16(xa[st][0], xoff, xb + sr * 128);
      gld16(xa[st][1], xoff, xb + sr * 128 + 8192);
    }
    TS(1);
    f32x4 acc[8], ssum = z4, ssq = z4, cs = z4;
#pragma unroll
    for (int n = 0; n < 8; ++n) acc[n] = z4;
    const int own = (w >> 2) * 8192 + (trb ^ ((w & 3) << 5));
#pragma unroll
    for (int st = 0; st < 8; ++st) {
      if (st == 0) wait_vm2<28>(xa[0][0], xa[0][1]); else if (st == 1) wait_vm2<24>(xa[1][0], xa[1][1]);
      else if (st == 2) wait_vm2<20>(xa[2][0], xa[2][1]); else if (st == 3) wait_vm2<16>(xa[3][0], xa[3][1]);
      else if (st == 4) wait_vm2<12>(xa[4][0], xa[4][1]); else if (st == 5) wait_vm2<8>(xa[5][0], xa[5][1]);
      else if (st == 6) wait_vm2<4>(xa[6][0], xa[6][1]); else wait_vm2<0>(xa[7][0], xa[7][1]);
      BAR();
      asm volatile("" ::: "memory");
      if (st == 1) TS(2);
      bf16x8 r[2];
      x_fix(xa[st][0], xa[st][1], r[0], r[1]);
#pragma unroll
      for (int k2 = 0; k2 < 2; ++k2) {
        const int base = st * 16384 + k2 * 4096;
        bf16x8 wf[8];
#pragma unroll
        for (int n = 0; n < 8; ++n) wf[n] = tr_frag(lds, base + (n >> 2) * 8192 + (trb ^ ((n & 3) << 5)));
        const bf16x8 wn = tr_frag(lds, base + own);
        ssum = mfma32(r[k2], ones, ssum);
        ssq = mfma32(r[k2], r[k2], ssq);
#pragma unroll
        for (int n = 0; n < 8; ++n) acc[n] = mfma32(r[k2], wf[n], acc[n]);
        cs = mfma32(ones, wn, cs);
      }
    }
    TS(3);
    if (g == 0) *(float*)(lds + K1_CSO + i * 32 + w * 4) = cs[0];
    float mean[4], rs[4];
#pragma unroll
    for (int r = 0; r < 4; ++r) {
      const float sq = bperm(20 * g + r, ssq[r]);
      mean[r] = ssum[r] * (1.0f / 512.0f);
      const float var = fmaxf(sq * (1.0f / 512.0f) - mean[r] * mean[r], 0.f);
      rs[r] = __builtin_amdgcn_rsqf(var + 1e-5f);
    }
    __syncthreads();
    const f32x4 csk = *(const f32x4*)(lds + K1_CSO + i * 32);
    const f32x4 csv = *(const f32x4*)(lds + K1_CSO + i * 32 + 16);
    const bf16x4 ones4 = as_bf4((u32x2){0x3f803f80u, 0x3f803f80u});
#pragma unroll
    for (int h = 0; h < 4; ++h) {
      float kk[4], vv[4];
#pragma unroll
      for (int r = 0; r < 4; ++r) {
        kk[r] = phi_f((acc[h][r] - mean[r] * csk[h]) * rs[r]);
        vv[r] = (acc[4 + h][r] - mean[r] * csv[h]) * rs[r];
      }
      const bf16x4 pk4 = as_bf4((u32x2){pk2(kk[0], kk[1]), pk2(kk[2], kk[3])});
      const bf16x4 v4 = as_bf4((u32x2){pk2(vv[0], vv[1]), pk2(vv[2], vv[3])});
      const f32x4 kvt = mfma16(pk4, v4, z4);
      const f32x4 zz = mfma16(pk4, ones4, z4);
      *(f32x4*)(lds + w * 4096 + h * 1024 + l * 16) = kvt;
      if (i == 0) *(f32x4*)(lds + 32768 + w * 256 + h * 64 + g * 16) = zz;
    }
    __syncthreads();
    if (tid < 256) {
      f32x4 s = *(const f32x4*)(lds + tid * 16);
#pragma unroll
      for (int ww = 1; ww < 8; ++ww) s += *(const f32x4*)(lds + ww * 4096 + tid * 16);
      *(f32x4*)(ws + WS_KVP + (size_t)(rt * 32 + 4 * j) * 1024 + tid * 16) = s;
    } else if (tid < 272) {
      const int t = tid - 256;
      f32x4 s = *(const f32x4*)(lds + 32768 + t * 16);
#pragma unroll
      for (int ww = 1; ww < 8; ++ww) s += *(const f32x4*)(lds + 32768 + ww * 256 + t * 16);
      *(f32x4*)(ws + WS_ZP + (size_t)(rt * 32 + 4 * j) * 64 + t * 16) = s;
    }
  } else {
    const int jq = j - 8;
    const char* wb = wqkv + (size_t)(w * 8) * 3072 + jq * 128;
#pragma unroll
    for (int st = 0; st < 8; ++st) {
      const int sr = (st + rot) & 7;
      glds16(woff, wb + (size_t)sr * (64 * 3072), lds0 + st * 8192 + w * 1024);
      gld16(xa[st][0], xoff, xb + sr * 128);
      gld16(xa[st][1], xoff, xb + sr * 128 + 8192);
    }
    const int qb = rt * 8 + jq;
    const char* ssrc;
    char* sdst;
    int gstr, tstr, rstr, uq;
    if (qb < 56) {
      uq = qb >> 1;
      const int s = (qb & 1) * 8 + w;
      ssrc = w1 + (size_t)(64 * (s >> 1) + 2 * (s & 1)) * 3584 + uq * 128;
      gstr = 16 * 3584; tstr = 3584; rstr = 4 * 3584;
      sdst = ws + WS_W1 + (size_t)(s * 112 + uq * 4) * 1024;
    } else if (qb < 72) {
      uq = (qb - 56) >> 1;
      const int s = (qb & 1) * 8 + w;
      ssrc = wo + (size_t)(32 * s) * 1024 + uq * 128;
      gstr = 4096; tstr = 16384; rstr = 1024;
      sdst = ws + WS_WO + (size_t)(uq * 16 + s) * 4096;
    } else {
      const int v = qb - 72;
      uq = v / 7;
      const int s = (v - uq * 7) * 8 + w;
      ssrc = w2 + (size_t)(64 * (s >> 1) + 2 * (s & 1)) * 1024 + uq * 128;
      gstr = 16384; tstr = 1024; rstr = 4096;
      sdst = ws + WS_W2 + (size_t)(uq * 56 + s) * 4096;
    }
    const unsigned soff = g * gstr + i * 8;
    u32x2 v8[8];
#pragma unroll
    for (int jj = 0; jj < 8; ++jj) gld8(v8[jj], soff, ssrc + (jj >> 2) * tstr + (jj & 3) * rstr);
    TS(1);
    f32x4 acc[4], ssum = z4, ssq = z4, cs = z4;
#pragma unroll
    for (int n = 0; n < 4; ++n) acc[n] = z4;
    const int own = trb ^ ((w & 3) << 5);
#pragma unroll
    for (int st = 0; st < 8; ++st) {
      if (st == 0) wait_vm2<29>(xa[0][0], xa[0][1]); else if (st == 1) wait_vm2<26>(xa[1][0], xa[1][1]);
      else if (st == 2) wait_vm2<23>(xa[2][0], xa[2][1]); else if (st == 3) wait_vm2<20>(xa[3][0], xa[3][1]);
      else if (st == 4) wait_vm2<17>(xa[4][0], xa[4][1]); else if (st == 5) wait_vm2<14>(xa[5][0], xa[5][1]);
      else if (st == 6) wait_vm2<11>(xa[6][0], xa[6][1]); else wait_vm2<8>(xa[7][0], xa[7][1]);
      BAR();
      asm volatile("" ::: "memory");
      if (st == 1) TS(2);
      bf16x8 r[2];
      x_fix(xa[st][0], xa[st][1], r[0], r[1]);
#pragma unroll
      for (int k2 = 0; k2 < 2; ++k2) {
        const int base = st * 8192 + k2 * 4096;
        bf16x8 wf[4];
#pragma unroll
        for (int n = 0; n < 4; ++n) wf[n] = tr_frag(lds, base + (trb ^ (n << 5)));
        const bf16x8 wn = tr_frag(lds, base + own);
        ssum = mfma32(ones, r[k2], ssum);
        ssq = mfma32(r[k2], r[k2], ssq);
#pragma unroll
        for (int n = 0; n < 4; ++n) acc[n] = mfma32(wf[n], r[k2], acc[n]);
        cs = mfma32(wn, ones, cs);
      }
    }
    TS(3);
    if (i == 0 && w < 4) *(f32x4*)(lds + K1_CSO + w * 64 + g * 16) = cs;
    const float sel = pp == 0 ? ssq[0] : (pp == 1 ? ssq[1] : (pp == 2 ? ssq[2] : ssq[3]));
    const float sq = bperm(16 * q4 + i, sel);
    const float mean = ssum[0] * (1.0f / 512.0f);
    const float var = fmaxf(sq * (1.0f / 512.0f) - mean * mean, 0.f);
    const float rs = __builtin_amdgcn_rsqf(var + 1e-5f);
    __syncthreads();
#pragma unroll
    for (int s = 0; s < 2; ++s) {
      u32x4 o;
#pragma unroll
      for (int n = 0; n < 2; ++n) {
        const f32x4 c4 = *(const f32x4*)(lds + K1_CSO + (2 * s + n) * 64 + g * 16);
        float v[4];
#pragma unroll
        for (int r = 0; r < 4; ++r) v[r] = phi_f((acc[2 * s + n][r] - mean * c4[r]) * rs);
        o[2 * n] = pk2(v[0], v[1]);
        o[2 * n + 1] = pk2(v[2], v[3]);
      }
      *(u32x4*)(ws + WS_PQ + (size_t)((rt * 8 + w) * 16 + 2 * jq + s) * 1024 + l * 16) = o;
    }
    asm volatile("s_waitcnt vmcnt(2)"
                 : "+v"(v8[0]), "+v"(v8[1]), "+v"(v8[2]), "+v"(v8[3]), "+v"(v8[4]), "+v"(v8[5]), "+v"(v8[6]), "+v"(v8[7])
                 :: "memory");
    u32x4 fo[4];
#pragma unroll
    for (int e = 0; e < 4; ++e) {
#pragma unroll
      for (int jj = 0; jj < 4; ++jj) {
        const unsigned a = v8[2 * jj][e >> 1], c = v8[2 * jj + 1][e >> 1];
        fo[e][jj] = (e & 1) ? perm_hi(a, c) : perm_lo(a, c);
      }
      *(u32x4*)(sdst + e * 1024 + l * 16) = fo[e];
    }
    if (qb < 56) {
#pragma unroll
      for (int e = 0; e < 4; ++e) {
        const f32x4 c = mfma32(as_bf8(fo[e]), ones, z4);
        if (i == 0) *(f32x4*)(lds + w * 256 + e * 64 + g * 16) = c;
      }
      __syncthreads();
      if (tid < 64) {
        float s = 0.f;
#pragma unroll
        for (int ww = 0; ww < 8; ++ww) s += *(const float*)(lds + ww * 256 + tid * 4);
        *(float*)(ws + WS_CS1 + (qb & 1) * 8192 + uq * 256 + tid * 4) = s;
      } else if (tid < 128 && (qb & 1) == 0) {
        const int t = tid - 64;
        *(float*)(ws + WS_CS1 + 16384 + uq * 256 + t * 4) =
            bflo(*(const unsigned short*)(b1 + (uq * 64 + 4 * (t & 15) + (t >> 4)) * 2));
      }
    }
  }
  TS_END(0, 4, ws);
}

__global__ __launch_bounds__(64) void kr_prep(char* __restrict__ ws) {
  TS_DECL
  const int l = threadIdx.x, g = l >> 4;
  const int h = blockIdx.x;
  f32x4 s = {0.f, 0.f, 0.f, 0.f}, z = {0.f, 0.f, 0.f, 0.f};
  f32x4 t[16], tz[16];
#pragma unroll
  for (int r = 0; r < 16; ++r) {
    t[r] = *(const f32x4*)(ws + WS_KVP + (size_t)(r * 32 + h) * 1024 + l * 16);
    tz[r] = *(const f32x4*)(ws + WS_ZP + (size_t)(r * 32 + h) * 64 + g * 16);
  }
#pragma unroll
  for (int r = 0; r < 16; ++r) { s += t[r]; z += tz[r]; }
  *(u32x2*)(ws + WS_KVF + (h >> 1) * 1024 + l * 16 + (h & 1) * 8) = (u32x2){pk2(s[0], s[1]), pk2(s[2], s[3])};
  *(u32x2*)(ws + WS_ZF + (h >> 1) * 1024 + l * 16 + (h & 1) * 8) = (u32x2){pk2(z[0], z[1]), pk2(z[2], z[3])};
  TS_END(1, 1, ws);
}

__global__ __launch_bounds__(256, 1) void k2_attn(const char* __restrict__ x, char* __restrict__ ws) {
  __shared__ __attribute__((aligned(16))) char lds[4 * 3 * 4096];
  TS_DECL
  const int tid = threadIdx.x, l = tid & 63, i = l & 15, g = l >> 4;
  const int w = __builtin_amdgcn_readfirstlane(tid >> 6);
  const int b = blockIdx.x, xcd = b & 7, idx = b >> 3;
  const int rt = xcd * 4 + (idx >> 3), q = idx & 7;

  u32x4 kvf[4], zf[4], pqf[4][4], wf[4][4];
#pragma unroll
  for (int s = 0; s < 4; ++s) {
    const int sg = 4 * w + s;
    kvf[s] = ldg16(ws + WS_KVF + sg * 1024 + l * 16);
    zf[s] = ldg16(ws + WS_ZF + sg * 1024 + l * 16);
#pragma unroll
    for (int m = 0; m < 4; ++m) pqf[s][m] = ldg16(ws + WS_PQ + (size_t)((rt * 4 + m) * 16 + sg) * 1024 + l * 16);
#pragma unroll
    for (int e = 0; e < 4; ++e) wf[s][e] = ldg16(ws + WS_WO + (size_t)((q * 16 + sg) * 4 + e) * 1024 + l * 16);
  }
  const int row = rt * 64 + w * 16 + i;
  const u32x4 xr0 = ldg16(x + (size_t)row * 1024 + q * 128 + g * 32);
  const u32x4 xr1 = ldg16(x + (size_t)row * 1024 + q * 128 + g * 32 + 16);

  f32x4 acc[4][4];
#pragma unroll
  for (int m = 0; m < 4; ++m)
#pragma unroll
    for (int e = 0; e < 4; ++e) acc[m][e] = (f32x4){0.f, 0.f, 0.f, 0.f};
  const f32x4 z4 = {0.f, 0.f, 0.f, 0.f};
#pragma unroll
  for (int s = 0; s < 4; ++s) {
#pragma unroll
    for (int m = 0; m < 4; ++m) {
      u32x4 of;
#pragma unroll
      for (int hh = 0; hh < 2; ++hh) {
        const bf16x4 pq = as_bf4((u32x2){pqf[s][m][2 * hh], pqf[s][m][2 * hh + 1]});
        const f32x4 num = mfma16(as_bf4((u32x2){kvf[s][2 * hh], kvf[s][2 * hh + 1]}), pq, z4);
        const f32x4 den = mfma16(as_bf4((u32x2){zf[s][2 * hh], zf[s][2 * hh + 1]}), pq, z4);
        const float rd = __builtin_amdgcn_rcpf(bfr(bfr(den[0]) + 1e-6f));
        of[2 * hh] = pk2(num[0] * rd, num[1] * rd);
        of[2 * hh + 1] = pk2(num[2] * rd, num[3] * rd);
      }
#pragma unroll
      for (int e = 0; e < 4; ++e) acc[m][e] = mfma32(as_bf8(wf[s][e]), as_bf8(of), acc[m][e]);
    }
  }
  TS(1);
#pragma unroll
  for (int m = 0; m < 4; ++m) {
    if (m != w) {
      const int slot = (w - m - 1) & 3;
#pragma unroll
      for (int e = 0; e < 4; ++e) *(f32x4*)(lds + (m * 3 + slot) * 4096 + e * 1024 + l * 16) = acc[m][e];
    }
  }
  __syncthreads();
  f32x4 fin[4];
#pragma unroll
  for (int e = 0; e < 4; ++e) fin[e] = w == 0 ? acc[0][e] : (w == 1 ? acc[1][e] : (w == 2 ? acc[2][e] : acc[3][e]));
#pragma unroll
  for (int sl = 0; sl < 3; ++sl)
#pragma unroll
    for (int e = 0; e < 4; ++e) fin[e] += *(const f32x4*)(lds + (w * 3 + sl) * 4096 + e * 1024 + l * 16);

  float hv[4][4];
  float sum = 0.f;
#pragma unroll
  for (int r = 0; r < 4; ++r) {
    const unsigned lo = r < 2 ? xr0[2 * r] : xr1[2 * r - 4];
    const unsigned hi = r < 2 ? xr0[2 * r + 1] : xr1[2 * r - 3];
    hv[0][r] = bfr(bflo(lo) + bfr(fin[0][r]));
    hv[1][r] = bfr(bfhi(lo) + bfr(fin[1][r]));
    hv[2][r] = bfr(bflo(hi) + bfr(fin[2][r]));
    hv[3][r] = bfr(bfhi(hi) + bfr(fin[3][r]));
    sum += (hv[0][r] + hv[1][r]) + (hv[2][r] + hv[3][r]);
  }
  const int mt = rt * 4 + w;
#pragma unroll
  for (int t = 0; t < 2; ++t) {
    u32x4 o = {pk2(hv[2 * t][0], hv[2 * t][1]), pk2(hv[2 * t][2], hv[2 * t][3]),
               pk2(hv[2 * t + 1][0], hv[2 * t + 1][1]), pk2(hv[2 * t + 1][2], hv[2 * t + 1][3])};
    *(u32x4*)(ws + WS_H + (size_t)(mt * 16 + 2 * q + t) * 1024 + l * 16) = o;
  }
  sum += __shfl_xor(sum, 16);
  sum += __shfl_xor(sum, 32);
  const float gm = sum * (1.0f / 64.0f);
  float m2 = 0.f;
#pragma unroll
  for (int e = 0; e < 4; ++e)
#pragma unroll
    for (int r = 0; r < 4; ++r) { const float d = hv[e][r] - gm; m2 += d * d; }
  m2 += __shfl_xor(m2, 16);
  m2 += __shfl_xor(m2, 32);
  if (g == 0) *(f32x2*)(ws + WS_HST + (size_t)row * 64 + q * 8) = (f32x2){gm, m2};
  TS_END(2, 2, ws);
}

__global__ __launch_bounds__(256, 1) void k3_up(const char* __restrict__ b1, char* __restrict__ ws) {
  __shared__ __attribute__((aligned(16))) char lds[112 * 1024];
  TS_DECL
  const int tid = threadIdx.x, l = tid & 63, i = l & 15, g = l >> 4;
  const int w = __builtin_amdgcn_readfirstlane(tid >> 6);
  const int b = blockIdx.x, xcd = b & 7, idx = b >> 3;
  const int rt = xcd * 2 + (idx >> 4), cb = idx & 15;
  const int mt0 = rt * 8 + w * 2;

  u32x4 xf[2][16];
  const char* wsrc = ws + WS_W1 + (size_t)(cb * 7) * 1024 + l * 16;
  const char* xsrc = ws + WS_H + (size_t)(mt0 * 16) * 1024 + l * 16;
#pragma unroll
  for (int st = 0; st < 4; ++st) {
    const int ks = 4 * st + w;
#pragma unroll
    for (int n = 0; n < 7; ++n)
      __builtin_amdgcn_global_load_lds((const unsigned*)(wsrc + (size_t)(ks * 112 + n) * 1024),
                                       (LDS_AS unsigned*)(lds + (ks * 7 + n) * 1024), 16, 0, 0);
#pragma unroll
    for (int k2 = 0; k2 < 4; ++k2) {
      xf[0][4 * st + k2] = ldg16(xsrc + (4 * st + k2) * 1024);
      xf[1][4 * st + k2] = ldg16(xsrc + (16 + 4 * st + k2) * 1024);
    }
  }
  f32x4 stp[2];
#pragma unroll
  for (int m = 0; m < 2; ++m) stp[m] = *(const f32x4*)(ws + WS_HST + (size_t)((mt0 + m) * 16 + i) * 64 + g * 16);

  TS(1);
  f32x4 acc[2][7];
#pragma unroll
  for (int m = 0; m < 2; ++m)
#pragma unroll
    for (int n = 0; n < 7; ++n) acc[m][n] = (f32x4){0.f, 0.f, 0.f, 0.f};

#pragma unroll
  for (int st = 0; st < 4; ++st) {
    if (st == 0) wait_vm<47>(); else if (st == 1) wait_vm<32>(); else if (st == 2) wait_vm<17>(); else wait_vm<2>();
    BAR();
    if (st == 1) TS(2);
#pragma unroll
    for (int k2 = 0; k2 < 4; ++k2) {
      const int ks = 4 * st + k2;
#pragma unroll
      for (int n = 0; n < 7; ++n) {
        const bf16x8 wv = *(const bf16x8*)(lds + (ks * 7 + n) * 1024 + l * 16);
        acc[0][n] = mfma32(wv, as_bf8(xf[0][ks]), acc[0][n]);
        acc[1][n] = mfma32(wv, as_bf8(xf[1][ks]), acc[1][n]);
      }
    }
  }

  TS(3);
  float la[2], lb[2];
#pragma unroll
  for (int m = 0; m < 2; ++m) {
    float s = stp[m][0] + stp[m][2];
    s += __shfl_xor(s, 16);
    s += __shfl_xor(s, 32);
    const float mean = s * 0.125f;
    const float d0 = stp[m][0] - mean, d1 = stp[m][2] - mean;
    float m2 = stp[m][1] + stp[m][3] + 64.f * (d0 * d0 + d1 * d1);
    m2 += __shfl_xor(m2, 16);
    m2 += __shfl_xor(m2, 32);
    const float var = m2 * (1.0f / 512.0f);
    const float mb = bfr(mean);
    const float r1 = 1.0f / bfr(__builtin_sqrtf(bfr(bfr(var) + 1e-5f)));
    const float mn2 = bfr((mean - mb) * r1);
    const float var2 = var * r1 * r1;
    const float r2 = 1.0f / bfr(__builtin_sqrtf(bfr(bfr(var2) + 1e-5f)));
    la[m] = r1 * r2;
    lb[m] = (mb * r1 + mn2) * r2;
  }
#pragma unroll
  for (int n = 0; n < 7; ++n) {
    const int f = cb * 7 + n;
    const f32x4 csv = *(const f32x4*)(ws + WS_CS1 + f * 64 + g * 16) + *(const f32x4*)(ws + WS_CS1 + 8192 + f * 64 + g * 16);
    const f32x4 bv = *(const f32x4*)(ws + WS_CS1 + 16384 + f * 64 + g * 16);
#pragma unroll
    for (int m = 0; m < 2; ++m) {
      float gl[4];
#pragma unroll
      for (int r = 0; r < 4; ++r) {
        const float t = acc[m][n][r] * la[m] + (bv[r] - lb[m] * csv[r]);
        const float u = t * (t * t * (-0.044715f * 2.302208198f) - 2.302208198f);
        gl[r] = t * __builtin_amdgcn_rcpf(1.0f + __builtin_amdgcn_exp2f(u));
      }
      *(u32x2*)(ws + WS_HM + (size_t)((mt0 + m) * 56 + (f >> 1)) * 1024 + l * 16 + (f & 1) * 8) =
          (u32x2){pk2(gl[0], gl[1]), pk2(gl[2], gl[3])};
    }
  }
  TS_END(3, 4, ws);
}

__global__ __launch_bounds__(256, 1) void k4_down(const char* __restrict__ b2, const char* __restrict__ ws,
                                                   char* __restrict__ out) {
  __shared__ __attribute__((aligned(16))) char lds[4 * 3 * 4096];
  TS_DECL
  const int tid = threadIdx.x, l = tid & 63, i = l & 15, g = l >> 4;
  const int w = __builtin_amdgcn_readfirstlane(tid >> 6);
  const int b = blockIdx.x, xcd = b & 7, idx = b >> 3;
  const int rt = xcd * 4 + (idx >> 3), q = idx & 7;

  const char* xsrc = ws + WS_HM + (size_t)((rt * 4) * 56 + 14 * w) * 1024 + l * 16;
  const char* wsrc = ws + WS_W2 + (size_t)((q * 56 + 14 * w) * 4) * 1024 + l * 16;
  u32x4 xf[7][4], wf[7][4];
#pragma unroll
  for (int s = 0; s < 7; ++s) {
#pragma unroll
    for (int m = 0; m < 4; ++m) xf[s][m] = ldg16(xsrc + (size_t)(m * 56 + s) * 1024);
#pragma unroll
    for (int e = 0; e < 4; ++e) wf[s][e] = ldg16(wsrc + (size_t)(s * 4 + e) * 1024);
  }
  f32x4 acc[4][4];
#pragma unroll
  for (int m = 0; m < 4; ++m)
#pragma unroll
    for (int e = 0; e < 4; ++e) acc[m][e] = (f32x4){0.f, 0.f, 0.f, 0.f};
#pragma unroll
  for (int s = 0; s < 7; ++s) {
#pragma unroll
    for (int m = 0; m < 4; ++m)
#pragma unroll
      for (int e = 0; e < 4; ++e) acc[m][e] = mfma32(as_bf8(wf[s][e]), as_bf8(xf[s][m]), acc[m][e]);
    __builtin_amdgcn_sched_barrier(0);
#pragma unroll
    for (int m = 0; m < 4; ++m) xf[s][m] = ldg16(xsrc + (size_t)(m * 56 + 7 + s) * 1024);
#pragma unroll
    for (int e = 0; e < 4; ++e) wf[s][e] = ldg16(wsrc + (size_t)((7 + s) * 4 + e) * 1024);
    __builtin_amdgcn_sched_barrier(0);
  }
  TS(1);
  const int mt = rt * 4 + w;
  const u32x4 h0 = ldg16(ws + WS_H + (size_t)(mt * 16 + 2 * q) * 1024 + l * 16);
  const u32x4 h1 = ldg16(ws + WS_H + (size_t)(mt * 16 + 2 * q + 1) * 1024 + l * 16);
  const u32x4 bb0 = ldg16(b2 + q * 128 + g * 32);
  const u32x4 bb1 = ldg16(b2 + q * 128 + g * 32 + 16);
#pragma unroll
  for (int s = 0; s < 7; ++s) {
#pragma unroll
    for (int m = 0; m < 4; ++m)
#pragma unroll
      for (int e = 0; e < 4; ++e) acc[m][e] = mfma32(as_bf8(wf[s][e]), as_bf8(xf[s][m]), acc[m][e]);
  }
  TS(2);
#pragma unroll
  for (int m = 0; m < 4; ++m) {
    if (m != w) {
      const int slot = (w - m - 1) & 3;
#pragma unroll
      for (int e = 0; e < 4; ++e) *(f32x4*)(lds + (m * 3 + slot) * 4096 + e * 1024 + l * 16) = acc[m][e];
    }
  }
  __syncthreads();
  f32x4 fin[4];
#pragma unroll
  for (int e = 0; e < 4; ++e) fin[e] = w == 0 ? acc[0][e] : (w == 1 ? acc[1][e] : (w == 2 ? acc[2][e] : acc[3][e]));
#pragma unroll
  for (int sl = 0; sl < 3; ++sl)
#pragma unroll
    for (int e = 0; e < 4; ++e) fin[e] += *(const f32x4*)(lds + (w * 3 + sl) * 4096 + e * 1024 + l * 16);

  float ov[4][4];
#pragma unroll
  for (int e = 0; e < 4; ++e) {
#pragma unroll
    for (int r = 0; r < 4; ++r) {
      const unsigned hu = (e < 2 ? h0 : h1)[2 * (e & 1) + (r >> 1)];
      const float hval = (r & 1) ? bfhi(hu) : bflo(hu);
      const unsigned bu = (r < 2 ? bb0 : bb1)[2 * (r & 1) + (e >> 1)];
      const float bval = (e & 1) ? bfhi(bu) : bflo(bu);
      ov[e][r] = hval + bfr(bfr(fin[e][r]) + bval);
    }
  }
  const int row = rt * 64 + w * 16 + i;
  u32x4 o0 = {pk2(ov[0][0], ov[1][0]), pk2(ov[2][0], ov[3][0]), pk2(ov[0][1], ov[1][1]), pk2(ov[2][1], ov[3][1])};
  u32x4 o1 = {pk2(ov[0][2], ov[1][2]), pk2(ov[2][2], ov[3][2]), pk2(ov[0][3], ov[1][3]), pk2(ov[2][3], ov[3][3])};
  *(u32x4*)(out + (size_t)row * 1024 + q * 128 + g * 32) = o0;
  *(u32x4*)(out + (size_t)row * 1024 + q * 128 + g * 32 + 16) = o1;
  TS_END(4, 3, ws);
}

extern "C" void kernel_launch(void* const* d_in, const int* in_sizes, int n_in, void* d_out, int out_size,
                              void* d_ws, size_t ws_size, hipStream_t stream) {
  const char* x = (const char*)d_in[0];
  const char* wqkv = (const char*)d_in[1];
  const char* wo = (const char*)d_in[2];
  const char* w1 = (const char*)d_in[4];
  const char* b1 = (const char*)d_in[5];
  const char* w2 = (const char*)d_in[6];
  const char* b2 = (const char*)d_in[7];
  char* ws = (char*)d_ws;
  k1_qkv<<<256, 512, 0, stream>>>(x, wqkv, wo, w1, w2, b1, ws);
  kr_prep<<<32, 64, 0, stream>>>(ws);
  k2_attn<<<256, 256, 0, stream>>>(x, ws);
  k3_up<<<256, 256, 0, stream>>>(b1, ws);
  k4_down<<<256, 256, 0, stream>>>(b2, ws, (char*)d_out);
}
```

```cpp
#include <hip/hip_runtime.h>
#include <hip/hip_bf16.h>
#include <stdint.h>

typedef __attribute__((ext_vector_type(8))) short bf16x8;
typedef __attribute__((ext_vector_type(4))) short bf16x4;
typedef __attribute__((ext_vector_type(4))) short v4i16_t;
typedef __attribute__((ext_vector_type(4))) float f32x4;
typedef __attribute__((ext_vector_type(2))) float f32x2;
typedef __attribute__((ext_vector_type(4))) unsigned u32x4;
typedef __attribute__((ext_vector_type(2))) unsigned u32x2;
typedef __attribute__((ext_vector_type(2))) __bf16 bfv2;

#define DEV __device__ __forceinline__
#define LDS_AS __attribute__((address_space(3)))

static constexpr size_t WS_PQ  = 0;
static constexpr size_t WS_H   = WS_PQ + (2u << 20);
static constexpr size_t WS_HM  = WS_H + (2u << 20);
static constexpr size_t WS_WO  = WS_HM + (7u << 20);
static constexpr size_t WS_W1  = WS_WO + (512u << 10);
static constexpr size_t WS_W2  = WS_W1 + (1792u << 10);
static constexpr size_t WS_KVP = WS_W2 + (1792u << 10);
static constexpr size_t WS_ZP  = WS_KVP + (512u << 10);
static constexpr size_t WS_KVF = WS_ZP + (32u << 10);
static constexpr size_t WS_ZF  = WS_KVF + (16u << 10);
static constexpr size_t WS_CS1 = WS_ZF + (16u << 10);
static constexpr size_t WS_HST = WS_CS1 + (24u << 10);

DEV f32x4 mfma32(bf16x8 a, bf16x8 b, f32x4 c) { return __builtin_amdgcn_mfma_f32_16x16x32_bf16(a, b, c, 0, 0, 0); }
DEV f32x4 mfma16(bf16x4 a, bf16x4 b, f32x4 c) { return __builtin_amdgcn_mfma_f32_16x16x16bf16_1k(a, b, c, 0, 0, 0); }

DEV unsigned pk2(float a, float b) {
  f32x2 v = {a, b};
  bfv2 r = __builtin_convertvector(v, bfv2);
  return __builtin_bit_cast(unsigned, r);
}
DEV float bflo(unsigned u) { return __builtin_bit_cast(float, u << 16); }
DEV float bfhi(unsigned u) { return __builtin_bit_cast(float, u & 0xffff0000u); }
DEV float bfr(float a) { return bflo(pk2(a, a)); }

DEV u32x4 ldg16(const void* p) { return *(const u32x4*)p; }
DEV u32x2 ldg8(const void* p) { return *(const u32x2*)p; }
DEV bf16x8 as_bf8(u32x4 v) { return __builtin_bit_cast(bf16x8, v); }
DEV bf16x4 as_bf4(u32x2 v) { return __builtin_bit_cast(bf16x4, v); }

DEV float phi_f(float t) {
  float e = __builtin_amdgcn_exp2f(t * 1.44269504088896341f);
  return (t > 0.f ? t + 1.f : e) + 1e-6f;
}

#define WAIT_V(n) asm volatile("s_waitcnt vmcnt(" #n ")" ::: "memory")
#define WAIT_L0() asm volatile("s_waitcnt lgkmcnt(0)" ::: "memory")
#define BAR() __builtin_amdgcn_s_barrier()

#define TS_DECL
#define TS(k)
#define TS_END(kid, n, wsp)
template <int N> DEV void wait_vm() { asm volatile("s_waitcnt vmcnt(%0)" ::"n"(N) : "memory"); }

DEV void ln_scalars(float mean, float var, float& a, float& b) {
  float mb = bfr(mean);
  float d = bfr(__builtin_sqrtf(bfr(bfr(var) + 1e-5f)));
  a = 1.0f / d;
  b = mb * a;
}

static constexpr int K1_CSO = 131072;
static constexpr int K1_LDS = K1_CSO + 2048;

DEV void glds16(unsigned voff, const char* sbase, unsigned ldsdst) {
  unsigned keep;
  asm volatile("s_mov_b32 %0, m0\n\ts_mov_b32 m0, %3\n\ts_nop 0\n\tglobal_load_lds_dwordx4 %1, %2\n\ts_mov_b32 m0, %0"
               : "=&s"(keep) : "v"(voff), "s"(sbase), "s"(ldsdst) : "memory");
}
DEV void gld16(u32x4& d, unsigned voff, const char* sbase) {
  asm volatile("global_load_dwordx4 %0, %1, %2" : "=v"(d) : "v"(voff), "s"(sbase) : "memory");
}
DEV void gld8(u32x2& d, unsigned voff, const char* sbase) {
  asm volatile("global_load_dwordx2 %0, %1, %2" : "=v"(d) : "v"(voff), "s"(sbase) : "memory");
}
template <int N> DEV void wait_vm2(u32x4& a, u32x4& b) {
  asm volatile("s_waitcnt vmcnt(%2)" : "+v"(a), "+v"(b) : "n"(N) : "memory");
}
DEV bf16x8 tr_frag(const char* lds, int off) {
  v4i16_t lo = __builtin_amdgcn_ds_read_tr16_b64_v4i16((LDS_AS v4i16_t*)(lds + off));
  v4i16_t hi = __builtin_amdgcn_ds_read_tr16_b64_v4i16((LDS_AS v4i16_t*)(lds + off + 512));
  return __builtin_shufflevector(lo, hi, 0, 1, 2, 3, 4, 5, 6, 7);
}
#define QT_OPS(P)                                                                                              \
  "v_cndmask_b32_dpp %0, %12, %8, vcc " P "\n\tv_cndmask_b32_dpp %1, %13, %9, vcc " P "\n\t"                   \
  "v_cndmask_b32_dpp %2, %14, %10, vcc " P "\n\tv_cndmask_b32_dpp %3, %15, %11, vcc " P "\n\t"                 \
  "v_cndmask_b32_dpp %4, %20, %16, vcc " P "\n\tv_cndmask_b32_dpp %5, %21, %17, vcc " P "\n\t"                 \
  "v_cndmask_b32_dpp %6, %22, %18, vcc " P "\n\tv_cndmask_b32_dpp %7, %23, %19, vcc " P
#define QT_STEP(MASK, P, TAIL, o0, k0, t0, o1, k1, t1)                                                         \
  asm volatile("s_mov_b32 vcc_lo, " MASK "\n\ts_mov_b32 vcc_hi, " MASK "\n\t" QT_OPS(P) TAIL                   \
               : "=&v"(o0[0]), "=&v"(o0[1]), "=&v"(o0[2]), "=&v"(o0[3]), "=&v"(o1[0]), "=&v"(o1[1]),           \
                 "=&v"(o1[2]), "=&v"(o1[3])                                                                    \
               : "v"(k0[0]), "v"(k0[1]), "v"(k0[2]), "v"(k0[3]), "v"(t0[0]), "v"(t0[1]), "v"(t0[2]),           \
                 "v"(t0[3]), "v"(k1[0]), "v"(k1[1]), "v"(k1[2]), "v"(k1[3]), "v"(t1[0]), "v"(t1[1]),           \
                 "v"(t1[2]), "v"(t1[3])                                                                        \
               : "vcc")
DEV void quad_tr(const u32x4 (&x)[4], bf16x8 (&y)[4]) {
  unsigned x0[4] = {x[0][0], x[0][1], x[0][2], x[0][3]}, x1[4] = {x[1][0], x[1][1], x[1][2], x[1][3]};
  unsigned x2[4] = {x[2][0], x[2][1], x[2][2], x[2][3]}, x3[4] = {x[3][0], x[3][1], x[3][2], x[3][3]};
  unsigned t0[4], t1[4], t2[4], t3[4], y0[4], y1[4], y2[4], y3[4];
  QT_STEP("0x55555555", "quad_perm:[1,0,3,2] row_mask:0xf bank_mask:0xf", "", t0, x0, x1, t2, x2, x3);
  QT_STEP("0xaaaaaaaa", "quad_perm:[1,0,3,2] row_mask:0xf bank_mask:0xf", "", t1, x1, x0, t3, x3, x2);
  QT_STEP("0x33333333", "quad_perm:[2,3,0,1] row_mask:0xf bank_mask:0xf", "\n\ts_nop 1", y0, t0, t2, y1, t1, t3);
  QT_STEP("0xcccccccc", "quad_perm:[2,3,0,1] row_mask:0xf bank_mask:0xf", "\n\ts_nop 1", y2, t2, t0, y3, t3, t1);
  y[0] = as_bf8((u32x4){y0[0], y0[1], y0[2], y0[3]});
  y[1] = as_bf8((u32x4){y1[0], y1[1], y1[2], y1[3]});
  y[2] = as_bf8((u32x4){y2[0], y2[1], y2[2], y2[3]});
  y[3] = as_bf8((u32x4){y3[0], y3[1], y3[2], y3[3]});
}
template <int N> DEV void wait_vm4(u32x4 (&a)[4]) {
  asm volatile("s_waitcnt vmcnt(%4)" : "+v"(a[0]), "+v"(a[1]), "+v"(a[2]), "+v"(a[3]) : "n"(N) : "memory");
}
#define SB0() __builtin_amdgcn_sched_barrier(0)
DEV unsigned perm_lo(unsigned a, unsigned b) { return __builtin_amdgcn_perm(b, a, 0x05040100u); }
DEV unsigned perm_hi(unsigned a, unsigned b) { return __builtin_amdgcn_perm(b, a, 0x07060302u); }
DEV float bperm(int lane, float v) {
  return __builtin_bit_cast(float, __builtin_amdgcn_ds_bpermute(lane * 4, __builtin_bit_cast(int, v)));
}

__global__ __launch_bounds__(512, 2) void k1_qkv(const char* __restrict__ x, const char* __restrict__ wqkv,
                                                  const char* __restrict__ wo, const char* __restrict__ w1,
                                                  const char* __restrict__ w2, const char* __restrict__ b1,
                                                  char* __restrict__ ws) {
  __shared__ __attribute__((aligned(16))) char lds[K1_LDS];
  TS_DECL
  const int tid = threadIdx.x, l = tid & 63, i = l & 15, g = l >> 4;
  const int w = __builtin_amdgcn_readfirstlane(tid >> 6);
  const int b = blockIdx.x, xcd = b & 7, idx = b >> 3;
  const int rt = xcd * 2 + (idx >> 4), j = idx & 15;
  const int rot = ((j >> 1) + 2 * (idx >> 4) + xcd) & 3;
  const unsigned lds0 = (unsigned)(uintptr_t)(LDS_AS char*)lds;

  const unsigned xoff = (i >> 2) * 4096 + g * 64 + (i & 3) * 16;
  const char* xb = x + (size_t)(rt * 128 + w * 16) * 1024;
  const int gkey = ((l >> 4) & 1) + 2 * ((w >> 1) & 1);
  const int wrg = (w >> 1) * 4 + (w & 1);
  const unsigned woff = (l >> 3) * 3072 + (((((l & 7) >> 1) ^ gkey) << 1) + (l & 1)) * 16;
  const int q4 = i >> 2, pp = i & 3;
  const int trb = (32 * g + q4) * 128 + (((q4 >> 1) & 1) + 2 * (g & 1)) * 32 + pp * 8;
  const u32x4 onesu = {0x3f803f80u, 0x3f803f80u, 0x3f803f80u, 0x3f803f80u};
  const bf16x8 ones = as_bf8(onesu);
  const f32x4 z4 = {0.f, 0.f, 0.f, 0.f};
  u32x4 xa[4][4];

  if (j < 8) {
    const char* wb = wqkv + (size_t)(wrg * 8) * 3072 + 1024 + j * 128;
#pragma unroll
    for (int st = 0; st < 4; ++st) {
      const int sr = (st + rot) & 3;
#pragma unroll
      for (int a = 0; a < 4; ++a) gld16(xa[st][a], xoff, xb + sr * 256 + a * 1024);
#pragma unroll
      for (int hh = 0; hh < 2; ++hh) {
        const char* src = wb + (size_t)(sr * 128 + hh * 16) * 3072;
        glds16(woff, src, lds0 + st * 32768 + (wrg + 2 * hh) * 1024);
        glds16(woff, src + 1024, lds0 + st * 32768 + 16384 + (wrg + 2 * hh) * 1024);
      }
    }
    TS(1);
    f32x4 acc[8], ssum = z4, ssq = z4, cs = z4;
#pragma unroll
    for (int n = 0; n < 8; ++n) acc[n] = z4;
    int ta[2][5];
#pragma unroll
    for (int n = 0; n < 4; ++n) ta[0][n] = trb ^ (n << 5);
    ta[0][4] = (w >> 2) * 16384 + (trb ^ ((w & 3) << 5));
#pragma unroll
    for (int n = 0; n < 5; ++n) {
      ta[1][n] = ta[0][n] + 65536;
      asm volatile("" : "+v"(ta[1][n]));
    }
    bf16x8 y[4], fa[9], fb[9];
#define KV_READ(F, HS, C)                                                                                      \
  _Pragma("unroll") for (int n = 0; n < 9; ++n) F[n] =                                                         \
      tr_frag(lds, ta[(HS) >> 2][n < 8 ? (n & 3) : 4] + (((HS) >> 1) & 1) * 32768 + (n < 8 ? (n >> 2) * 16384 : 0) + (C) * 1024)
#define KV_MMA(F, Y)                                                                                           \
  ssum = mfma32(Y, ones, ssum);                                                                                \
  ssq = mfma32(Y, Y, ssq);                                                                                     \
  _Pragma("unroll") for (int n = 0; n < 8; ++n) acc[n] = mfma32(Y, F[n], acc[n]);                              \
  cs = mfma32(ones, F[8], cs)
    bf16x8 yb;
#pragma unroll
    for (int hs = 0; hs < 8; ++hs) {
      const int st = hs >> 1;
      if (hs == 0) wait_vm4<26>(xa[0]); else if (hs == 1) wait_vm<24>();
      else if (hs == 2) wait_vm4<18>(xa[1]); else if (hs == 3) wait_vm<16>();
      else if (hs == 4) wait_vm4<10>(xa[2]); else if (hs == 5) wait_vm<8>();
      else if (hs == 6) wait_vm4<2>(xa[3]); else wait_vm<0>();
      BAR();
      asm volatile("" ::: "memory");
      if (hs == 2) TS(2);
      SB0();
      KV_READ(fa, hs, 2 * (hs & 1));
      SB0();
      if (hs > 0) { KV_MMA(fb, yb); }
      if ((hs & 1) == 0) quad_tr(xa[st], y);
      SB0();
      KV_READ(fb, hs, 2 * (hs & 1) + 1);
      SB0();
      KV_MMA(fa, y[2 * (hs & 1)]);
      yb = y[2 * (hs & 1) + 1];
      SB0();
    }
    KV_MMA(fb, yb);
    TS(3);
    if (g == 0) *(float*)(lds + K1_CSO + i * 32 + w * 4) = cs[0];
    float mean[4], rs[4];
#pragma unroll
    for (int r = 0; r < 4; ++r) {
      const float sq = bperm(20 * g + r, ssq[r]);
      mean[r] = ssum[r] * (1.0f / 512.0f);
      const float var = fmaxf(sq * (1.0f / 512.0f) - mean[r] * mean[r], 0.f);
      rs[r] = __builtin_amdgcn_rsqf(var + 1e-5f);
    }
    __syncthreads();
    const f32x4 csk = *(const f32x4*)(lds + K1_CSO + i * 32);
    const f32x4 csv = *(const f32x4*)(lds + K1_CSO + i * 32 + 16);
    const bf16x4 ones4 = as_bf4((u32x2){0x3f803f80u, 0x3f803f80u});
#pragma unroll
    for (int h = 0; h < 4; ++h) {
      float kk[4], vv[4];
#pragma unroll
      for (int r = 0; r < 4; ++r) {
        kk[r] = phi_f((acc[h][r] - mean[r] * csk[h]) * rs[r]);
        vv[r] = (acc[4 + h][r] - mean[r] * csv[h]) * rs[r];
      }
      const bf16x4 pk4 = as_bf4((u32x2){pk2(kk[0], kk[1]), pk2(kk[2], kk[3])});
      const bf16x4 v4 = as_bf4((u32x2){pk2(vv[0], vv[1]), pk2(vv[2], vv[3])});
      const f32x4 kvt = mfma16(pk4, v4, z4);
      const f32x4 zz = mfma16(pk4, ones4, z4);
      *(f32x4*)(lds + w * 4096 + h * 1024 + l * 16) = kvt;
      if (i == 0) *(f32x4*)(lds + 32768 + w * 256 + h * 64 + g * 16) = zz;
    }
    __syncthreads();
    if (tid < 256) {
      f32x4 s = *(const f32x4*)(lds + tid * 16);
#pragma unroll
      for (int ww = 1; ww < 8; ++ww) s += *(const f32x4*)(lds + ww * 4096 + tid * 16);
      *(f32x4*)(ws + WS_KVP + (size_t)(rt * 32 + 4 * j) * 1024 + tid * 16) = s;
    } else if (tid < 272) {
      const int t = tid - 256;
      f32x4 s = *(const f32x4*)(lds + 32768 + t * 16);
#pragma unroll
      for (int ww = 1; ww < 8; ++ww) s += *(const f32x4*)(lds + 32768 + ww * 256 + t * 16);
      *(f32x4*)(ws + WS_ZP + (size_t)(rt * 32 + 4 * j) * 64 + t * 16) = s;
    }
  } else {
    const int jq = j - 8;
    const char* wb = wqkv + (size_t)(wrg * 8) * 3072 + jq * 128;
#pragma unroll
    for (int st = 0; st < 4; ++st) {
      const int sr = (st + rot) & 3;
#pragma unroll
      for (int a = 0; a < 4; ++a) gld16(xa[st][a], xoff, xb + sr * 256 + a * 1024);
#pragma unroll
      for (int hh = 0; hh < 2; ++hh)
        glds16(woff, wb + (size_t)(sr * 128 + hh * 16) * 3072, lds0 + st * 16384 + (wrg + 2 * hh) * 1024);
    }
    const int qb = rt * 8 + jq;
    const char* ssrc;
    char* sdst;
    int gstr, tstr, rstr, uq;
    if (qb < 56) {
      uq = qb >> 1;
      const int s = (qb & 1) * 8 + w;
      ssrc = w1 + (size_t)(64 * (s >> 1) + 2 * (s & 1)) * 3584 + uq * 128;
      gstr = 16 * 3584; tstr = 3584; rstr = 4 * 3584;
      sdst = ws + WS_W1 + (size_t)(s * 112 + uq * 4) * 1024;
    } else if (qb < 72) {
      uq = (qb - 56) >> 1;
      const int s = (qb & 1) * 8 + w;
      ssrc = wo + (size_t)(32 * s) * 1024 + uq * 128;
      gstr = 4096; tstr = 16384; rstr = 1024;
      sdst = ws + WS_WO + (size_t)(uq * 16 + s) * 4096;
    } else {
      const int v = qb - 72;
      uq = v / 7;
      const int s = (v - uq * 7) * 8 + w;
      ssrc = w2 + (size_t)(64 * (s >> 1) + 2 * (s & 1)) * 1024 + uq * 128;
      gstr = 16384; tstr = 1024; rstr = 4096;
      sdst = ws + WS_W2 + (size_t)(uq * 56 + s) * 4096;
    }
    const unsigned soff = g * gstr + i * 8;
    u32x2 v8[8];
#pragma unroll
    for (int jj = 0; jj < 8; ++jj) gld8(v8[jj], soff, ssrc + (jj >> 2) * tstr + (jj & 3) * rstr);
    TS(1);
    f32x4 acc[4], ssum = z4, ssq = z4, cs = z4;
#pragma unroll
    for (int n = 0; n < 4; ++n) acc[n] = z4;
    int ta[5];
#pragma unroll
    for (int n = 0; n < 4; ++n) ta[n] = trb ^ (n << 5);
    ta[4] = trb ^ ((w & 3) << 5);
    bf16x8 y[4], fa[5], fb[5], yb;
#define Q_READ(F, HS, C)                                                                                       \
  _Pragma("unroll") for (int n = 0; n < 5; ++n) F[n] = tr_frag(lds, ta[n] + ((HS) >> 1) * 16384 + (C) * 1024)
#define Q_MMA(F, Y)                                                                                            \
  ssum = mfma32(ones, Y, ssum);                                                                                \
  ssq = mfma32(Y, Y, ssq);                                                                                     \
  _Pragma("unroll") for (int n = 0; n < 4; ++n) acc[n] = mfma32(F[n], Y, acc[n]);                              \
  cs = mfma32(F[4], ones, cs)
#pragma unroll
    for (int hs = 0; hs < 8; ++hs) {
      const int st = hs >> 1;
      if (hs == 0) wait_vm4<27>(xa[0]); else if (hs == 1) wait_vm<26>();
      else if (hs == 2) wait_vm4<21>(xa[1]); else if (hs == 3) wait_vm<20>();
      else if (hs == 4) wait_vm4<15>(xa[2]); else if (hs == 5) wait_vm<14>();
      else if (hs == 6) wait_vm4<9>(xa[3]); else wait_vm<8>();
      BAR();
      asm volatile("" ::: "memory");
      if (hs == 2) TS(2);
      SB0();
      Q_READ(fa, hs, 2 * (hs & 1));
      SB0();
      if (hs > 0) { Q_MMA(fb, yb); }
      if ((hs & 1) == 0) quad_tr(xa[st], y);
      SB0();
      Q_READ(fb, hs, 2 * (hs & 1) + 1);
      SB0();
      Q_MMA(fa, y[2 * (hs & 1)]);
      yb = y[2 * (hs & 1) + 1];
      SB0();
    }
    Q_MMA(fb, yb);
    TS(3);
    if (i == 0 && w < 4) *(f32x4*)(lds + K1_CSO + w * 64 + g * 16) = cs;
    const float sel = pp == 0 ? ssq[0] : (pp == 1 ? ssq[1] : (pp == 2 ? ssq[2] : ssq[3]));
    const float sq = bperm(16 * q4 + i, sel);
    const float mean = ssum[0] * (1.0f / 512.0f);
    const float var = fmaxf(sq * (1.0f / 512.0f) - mean * mean, 0.f);
    const float rs = __builtin_amdgcn_rsqf(var + 1e-5f);
    __syncthreads();
#pragma unroll
    for (int s = 0; s < 2; ++s) {
      u32x4 o;
#pragma unroll
      for (int n = 0; n < 2; ++n) {
        const f32x4 c4 = *(const f32x4*)(lds + K1_CSO + (2 * s + n) * 64 + g * 16);
        float v[4];
#pragma unroll
        for (int r = 0; r < 4; ++r) v[r] = phi_f((acc[2 * s + n][r] - mean * c4[r]) * rs);
        o[2 * n] = pk2(v[0], v[1]);
        o[2 * n + 1] = pk2(v[2], v[3]);
      }
      *(u32x4*)(ws + WS_PQ + (size_t)((rt * 8 + w) * 16 + 2 * jq + s) * 1024 + l * 16) = o;
    }
    asm volatile("s_waitcnt vmcnt(2)"
                 : "+v"(v8[0]), "+v"(v8[1]), "+v"(v8[2]), "+v"(v8[3]), "+v"(v8[4]), "+v"(v8[5]), "+v"(v8[6]), "+v"(v8[7])
                 :: "memory");
    u32x4 fo[4];
#pragma unroll
    for (int e = 0; e < 4; ++e) {
#pragma unroll
      for (int jj = 0; jj < 4; ++jj) {
        const unsigned a = v8[2 * jj][e >> 1], c = v8[2 * jj + 1][e >> 1];
        fo[e][jj] = (e & 1) ? perm_hi(a, c) : perm_lo(a, c);
      }
      *(u32x4*)(sdst + e * 1024 + l * 16) = fo[e];
    }
    if (qb < 56) {
#pragma unroll
      for (int e = 0; e < 4; ++e) {
        const f32x4 c = mfma32(as_bf8(fo[e]), ones, z4);
        if (i == 0) *(f32x4*)(lds + w * 256 + e * 64 + g * 16) = c;
      }
      __syncthreads();
      if (tid < 64) {
        float s = 0.f;
#pragma unroll
        for (int ww = 0; ww < 8; ++ww) s += *(const float*)(lds + ww * 256 + tid * 4);
        *(float*)(ws + WS_CS1 + (qb & 1) * 8192 + uq * 256 + tid * 4) = s;
      } else if (tid < 128 && (qb & 1) == 0) {
        const int t = tid - 64;
        *(float*)(ws + WS_CS1 + 16384 + uq * 256 + t * 4) =
            bflo(*(const unsigned short*)(b1 + (uq * 64 + 4 * (t & 15) + (t >> 4)) * 2));
      }
    }
  }
  TS_END(0, 4, ws);
}

__global__ __launch_bounds__(64) void kr_prep(char* __restrict__ ws) {
  TS_DECL
  const int l = threadIdx.x, g = l >> 4;
  const int h = blockIdx.x;
  f32x4 s = {0.f, 0.f, 0.f, 0.f}, z = {0.f, 0.f, 0.f, 0.f};
  f32x4 t[16], tz[16];
#pragma unroll
  for (int r = 0; r < 16; ++r) {
    t[r] = *(const f32x4*)(ws + WS_KVP + (size_t)(r * 32 + h) * 1024 + l * 16);
    tz[r] = *(const f32x4*)(ws + WS_ZP + (size_t)(r * 32 + h) * 64 + g * 16);
  }
#pragma unroll
  for (int r = 0; r < 16; ++r) { s += t[r]; z += tz[r]; }
  *(u32x2*)(ws + WS_KVF + (h >> 1) * 1024 + l * 16 + (h & 1) * 8) = (u32x2){pk2(s[0], s[1]), pk2(s[2], s[3])};
  *(u32x2*)(ws + WS_ZF + (h >> 1) * 1024 + l * 16 + (h & 1) * 8) = (u32x2){pk2(z[0], z[1]), pk2(z[2], z[3])};
  TS_END(1, 1, ws);
}

__global__ __launch_bounds__(256, 1) void k2_attn(const char* __restrict__ x, char* __restrict__ ws) {
  __shared__ __attribute__((aligned(16))) char lds[4 * 3 * 4096];
  TS_DECL
  const int tid = threadIdx.x, l = tid & 63, i = l & 15, g = l >> 4;
  const int w = __builtin_amdgcn_readfirstlane(tid >> 6);
  const int b = blockIdx.x, xcd = b & 7, idx = b >> 3;
  const int rt = xcd * 4 + (idx >> 3), q = idx & 7;

  u32x4 kvf[4], zf[4], pqf[4][4], wf[4][4];
#pragma unroll
  for (int s = 0; s < 4; ++s) {
    const int sg = 4 * w + s;
    kvf[s] = ldg16(ws + WS_KVF + sg * 1024 + l * 16);
    zf[s] = ldg16(ws + WS_ZF + sg * 1024 + l * 16);
#pragma unroll
    for (int m = 0; m < 4; ++m) pqf[s][m] = ldg16(ws + WS_PQ + (size_t)((rt * 4 + m) * 16 + sg) * 1024 + l * 16);
#pragma unroll
    for (int e = 0; e < 4; ++e) wf[s][e] = ldg16(ws + WS_WO + (size_t)((q * 16 + sg) * 4 + e) * 1024 + l * 16);
  }
  const int row = rt * 64 + w * 16 + i;
  const u32x4 xr0 = ldg16(x + (size_t)row * 1024 + q * 128 + g * 32);
  const u32x4 xr1 = ldg16(x + (size_t)row * 1024 + q * 128 + g * 32 + 16);

  f32x4 acc[4][4];
#pragma unroll
  for (int m = 0; m < 4; ++m)
#pragma unroll
    for (int e = 0; e < 4; ++e) acc[m][e] = (f32x4){0.f, 0.f, 0.f, 0.f};
  const f32x4 z4 = {0.f, 0.f, 0.f, 0.f};
#pragma unroll
  for (int s = 0; s < 4; ++s) {
#pragma unroll
    for (int m = 0; m < 4; ++m) {
      u32x4 of;
#pragma unroll
      for (int hh = 0; hh < 2; ++hh) {
        const bf16x4 pq = as_bf4((u32x2){pqf[s][m][2 * hh], pqf[s][m][2 * hh + 1]});
        const f32x4 num = mfma16(as_bf4((u32x2){kvf[s][2 * hh], kvf[s][2 * hh + 1]}), pq, z4);
        const f32x4 den = mfma16(as_bf4((u32x2){zf[s][2 * hh], zf[s][2 * hh + 1]}), pq, z4);
        const float rd = __builtin_amdgcn_rcpf(bfr(bfr(den[0]) + 1e-6f));
        of[2 * hh] = pk2(num[0] * rd, num[1] * rd);
        of[2 * hh + 1] = pk2(num[2] * rd, num[3] * rd);
      }
#pragma unroll
      for (int e = 0; e < 4; ++e) acc[m][e] = mfma32(as_bf8(wf[s][e]), as_bf8(of), acc[m][e]);
    }
  }
  TS(1);
#pragma unroll
  for (int m = 0; m < 4; ++m) {
    if (m != w) {
      const int slot = (w - m - 1) & 3;
#pragma unroll
      for (int e = 0; e < 4; ++e) *(f32x4*)(lds + (m * 3 + slot) * 4096 + e * 1024 + l * 16) = acc[m][e];
    }
  }
  __syncthreads();
  f32x4 fin[4];
#pragma unroll
  for (int e = 0; e < 4; ++e) fin[e] = w == 0 ? acc[0][e] : (w == 1 ? acc[1][e] : (w == 2 ? acc[2][e] : acc[3][e]));
#pragma unroll
  for (int sl = 0; sl < 3; ++sl)
#pragma unroll
    for (int e = 0; e < 4; ++e) fin[e] += *(const f32x4*)(lds + (w * 3 + sl) * 4096 + e * 1024 + l * 16);

  float hv[4][4];
  float sum = 0.f;
#pragma unroll
  for (int r = 0; r < 4; ++r) {
    const unsigned lo = r < 2 ? xr0[2 * r] : xr1[2 * r - 4];
    const unsigned hi = r < 2 ? xr0[2 * r + 1] : xr1[2 * r - 3];
    hv[0][r] = bfr(bflo(lo) + bfr(fin[0][r]));
    hv[1][r] = bfr(bfhi(lo) + bfr(fin[1][r]));
    hv[2][r] = bfr(bflo(hi) + bfr(fin[2][r]));
    hv[3][r] = bfr(bfhi(hi) + bfr(fin[3][r]));
    sum += (hv[0][r] + hv[1][r]) + (hv[2][r] + hv[3][r]);
  }
  const int mt = rt * 4 + w;
#pragma unroll
  for (int t = 0; t < 2; ++t) {
    u32x4 o = {pk2(hv[2 * t][0], hv[2 * t][1]), pk2(hv[2 * t][2], hv[2 * t][3]),
               pk2(hv[2 * t + 1][0], hv[2 * t + 1][1]), pk2(hv[2 * t + 1][2], hv[2 * t + 1][3])};
    *(u32x4*)(ws + WS_H + (size_t)(mt * 16 + 2 * q + t) * 1024 + l * 16) = o;
  }
  sum += __shfl_xor(sum, 16);
  sum += __shfl_xor(sum, 32);
  const float gm = sum * (1.0f / 64.0f);
  float m2 = 0.f;
#pragma unroll
  for (int e = 0; e < 4; ++e)
#pragma unroll
    for (int r = 0; r < 4; ++r) { const float d = hv[e][r] - gm; m2 += d * d; }
  m2 += __shfl_xor(m2, 16);
  m2 += __shfl_xor(m2, 32);
  if (g == 0) *(f32x2*)(ws + WS_HST + (size_t)row * 64 + q * 8) = (f32x2){gm, m2};
  TS_END(2, 2, ws);
}

__global__ __launch_bounds__(256, 1) void k3_up(const char* __restrict__ b1, char* __restrict__ ws) {
  __shared__ __attribute__((aligned(16))) char lds[112 * 1024];
  TS_DECL
  const int tid = threadIdx.x, l = tid & 63, i = l & 15, g = l >> 4;
  const int w = __builtin_amdgcn_readfirstlane(tid >> 6);
  const int b = blockIdx.x, xcd = b & 7, idx = b >> 3;
  const int rt = xcd * 2 + (idx >> 4), cb = idx & 15;
  const int mt0 = rt * 8 + w * 2;

  u32x4 xf[2][16];
  const char* wsrc = ws + WS_W1 + (size_t)(cb * 7) * 1024 + l * 16;
  const char* xsrc = ws + WS_H + (size_t)(mt0 * 16) * 1024 + l * 16;
#pragma unroll
  for (int st = 0; st < 4; ++st) {
    const int ks = 4 * st + w;
#pragma unroll
    for (int n = 0; n < 7; ++n)
      __builtin_amdgcn_global_load_lds((const unsigned*)(wsrc + (size_t)(ks * 112 + n) * 1024),
                                       (LDS_AS unsigned*)(lds + (ks * 7 + n) * 1024), 16, 0, 0);
#pragma unroll
    for (int k2 = 0; k2 < 4; ++k2) {
      xf[0][4 * st + k2] = ldg16(xsrc + (4 * st + k2) * 1024);
      xf[1][4 * st + k2] = ldg16(xsrc + (16 + 4 * st + k2) * 1024);
    }
  }
  f32x4 stp[2];
#pragma unroll
  for (int m = 0; m < 2; ++m) stp[m] = *(const f32x4*)(ws + WS_HST + (size_t)((mt0 + m) * 16 + i) * 64 + g * 16);

  TS(1);
  f32x4 acc[2][7];
#pragma unroll
  for (int m = 0; m < 2; ++m)
#pragma unroll
    for (int n = 0; n < 7; ++n) acc[m][n] = (f32x4){0.f, 0.f, 0.f, 0.f};

#pragma unroll
  for (int st = 0; st < 4; ++st) {
    if (st == 0) wait_vm<47>(); else if (st == 1) wait_vm<32>(); else if (st == 2) wait_vm<17>(); else wait_vm<2>();
    BAR();
    if (st == 1) TS(2);
#pragma unroll
    for (int k2 = 0; k2 < 4; ++k2) {
      const int ks = 4 * st + k2;
#pragma unroll
      for (int n = 0; n < 7; ++n) {
        const bf16x8 wv = *(const bf16x8*)(lds + (ks * 7 + n) * 1024 + l * 16);
        acc[0][n] = mfma32(wv, as_bf8(xf[0][ks]), acc[0][n]);
        acc[1][n] = mfma32(wv, as_bf8(xf[1][ks]), acc[1][n]);
      }
    }
  }

  TS(3);
  float la[2], lb[2];
#pragma unroll
  for (int m = 0; m < 2; ++m) {
    float s = stp[m][0] + stp[m][2];
    s += __shfl_xor(s, 16);
    s += __shfl_xor(s, 32);
    const float mean = s * 0.125f;
    const float d0 = stp[m][0] - mean, d1 = stp[m][2] - mean;
    float m2 = stp[m][1] + stp[m][3] + 64.f * (d0 * d0 + d1 * d1);
    m2 += __shfl_xor(m2, 16);
    m2 += __shfl_xor(m2, 32);
    const float var = m2 * (1.0f / 512.0f);
    const float mb = bfr(mean);
    const float r1 = 1.0f / bfr(__builtin_sqrtf(bfr(bfr(var) + 1e-5f)));
    const float mn2 = bfr((mean - mb) * r1);
    const float var2 = var * r1 * r1;
    const float r2 = 1.0f / bfr(__builtin_sqrtf(bfr(bfr(var2) + 1e-5f)));
    la[m] = r1 * r2;
    lb[m] = (mb * r1 + mn2) * r2;
  }
#pragma unroll
  for (int n = 0; n < 7; ++n) {
    const int f = cb * 7 + n;
    const f32x4 csv = *(const f32x4*)(ws + WS_CS1 + f * 64 + g * 16) + *(const f32x4*)(ws + WS_CS1 + 8192 + f * 64 + g * 16);
    const f32x4 bv = *(const f32x4*)(ws + WS_CS1 + 16384 + f * 64 + g * 16);
#pragma unroll
    for (int m = 0; m < 2; ++m) {
      float gl[4];
#pragma unroll
      for (int r = 0; r < 4; ++r) {
        const float t = acc[m][n][r] * la[m] + (bv[r] - lb[m] * csv[r]);
        const float u = t * (t * t * (-0.044715f * 2.302208198f) - 2.302208198f);
        gl[r] = t * __builtin_amdgcn_rcpf(1.0f + __builtin_amdgcn_exp2f(u));
      }
      *(u32x2*)(ws + WS_HM + (size_t)((mt0 + m) * 56 + (f >> 1)) * 1024 + l * 16 + (f & 1) * 8) =
          (u32x2){pk2(gl[0], gl[1]), pk2(gl[2], gl[3])};
    }
  }
  TS_END(3, 4, ws);
}

__global__ __launch_bounds__(256, 1) void k4_down(const char* __restrict__ b2, const char* __restrict__ ws,
                                                   char* __restrict__ out) {
  __shared__ __attribute__((aligned(16))) char lds[4 * 3 * 4096];
  TS_DECL
  const int tid = threadIdx.x, l = tid & 63, i = l & 15, g = l >> 4;
  const int w = __builtin_amdgcn_readfirstlane(tid >> 6);
  const int b = blockIdx.x, xcd = b & 7, idx = b >> 3;
  const int rt = xcd * 4 + (idx >> 3), q = idx & 7;

  const char* xsrc = ws + WS_HM + (size_t)((rt * 4) * 56 + 14 * w) * 1024 + l * 16;
  const char* wsrc = ws + WS_W2 + (size_t)((q * 56 + 14 * w) * 4) * 1024 + l * 16;
  u32x4 xf[7][4], wf[7][4];
#pragma unroll
  for (int s = 0; s < 7; ++s) {
#pragma unroll
    for (int m = 0; m < 4; ++m) xf[s][m] = ldg16(xsrc + (size_t)(m * 56 + s) * 1024);
#pragma unroll
    for (int e = 0; e < 4; ++e) wf[s][e] = ldg16(wsrc + (size_t)(s * 4 + e) * 1024);
  }
  f32x4 acc[4][4];
#pragma unroll
  for (int m = 0; m < 4; ++m)
#pragma unroll
    for (int e = 0; e < 4; ++e) acc[m][e] = (f32x4){0.f, 0.f, 0.f, 0.f};
#pragma unroll
  for (int s = 0; s < 7; ++s) {
#pragma unroll
    for (int m = 0; m < 4; ++m)
#pragma unroll
      for (int e = 0; e < 4; ++e) acc[m][e] = mfma32(as_bf8(wf[s][e]), as_bf8(xf[s][m]), acc[m][e]);
    __builtin_amdgcn_sched_barrier(0);
#pragma unroll
    for (int m = 0; m < 4; ++m) xf[s][m] = ldg16(xsrc + (size_t)(m * 56 + 7 + s) * 1024);
#pragma unroll
    for (int e = 0; e < 4; ++e) wf[s][e] = ldg16(wsrc + (size_t)((7 + s) * 4 + e) * 1024);
    __builtin_amdgcn_sched_barrier(0);
  }
  TS(1);
  const int mt = rt * 4 + w;
  const u32x4 h0 = ldg16(ws + WS_H + (size_t)(mt * 16 + 2 * q) * 1024 + l * 16);
  const u32x4 h1 = ldg16(ws + WS_H + (size_t)(mt * 16 + 2 * q + 1) * 1024 + l * 16);
  const u32x4 bb0 = ldg16(b2 + q * 128 + g * 32);
  const u32x4 bb1 = ldg16(b2 + q * 128 + g * 32 + 16);
#pragma unroll
  for (int s = 0; s < 7; ++s) {
#pragma unroll
    for (int m = 0; m < 4; ++m)
#pragma unroll
      for (int e = 0; e < 4; ++e) acc[m][e] = mfma32(as_bf8(wf[s][e]), as_bf8(xf[s][m]), acc[m][e]);
  }
  TS(2);
#pragma unroll
  for (int m = 0; m < 4; ++m) {
    if (m != w) {
      const int slot = (w - m - 1) & 3;
#pragma unroll
      for (int e = 0; e < 4; ++e) *(f32x4*)(lds + (m * 3 + slot) * 4096 + e * 1024 + l * 16) = acc[m][e];
    }
  }
  __syncthreads();
  f32x4 fin[4];
#pragma unroll
  for (int e = 0; e < 4; ++e) fin[e] = w == 0 ? acc[0][e] : (w == 1 ? acc[1][e] : (w == 2 ? acc[2][e] : acc[3][e]));
#pragma unroll
  for (int sl = 0; sl < 3; ++sl)
#pragma unroll
    for (int e = 0; e < 4; ++e) fin[e] += *(const f32x4*)(lds + (w * 3 + sl) * 4096 + e * 1024 + l * 16);

  float ov[4][4];
#pragma unroll
  for (int e = 0; e < 4; ++e) {
#pragma unroll
    for (int r = 0; r < 4; ++r) {
      const unsigned hu = (e < 2 ? h0 : h1)[2 * (e & 1) + (r >> 1)];
      const float hval = (r & 1) ? bfhi(hu) : bflo(hu);
      const unsigned bu = (r < 2 ? bb0 : bb1)[2 * (r & 1) + (e >> 1)];
      const float bval = (e & 1) ? bfhi(bu) : bflo(bu);
      ov[e][r] = hval + bfr(bfr(fin[e][r]) + bval);
    }
  }
  const int row = rt * 64 + w * 16 + i;
  u32x4 o0 = {pk2(ov[0][0], ov[1][0]), pk2(ov[2][0], ov[3][0]), pk2(ov[0][1], ov[1][1]), pk2(ov[2][1], ov[3][1])};
  u32x4 o1 = {pk2(ov[0][2], ov[1][2]), pk2(ov[2][2], ov[3][2]), pk2(ov[0][3], ov[1][3]), pk2(ov[2][3], ov[3][3])};
  *(u32x4*)(out + (size_t)row * 1024 + q * 128 + g * 32) = o0;
  *(u32x4*)(out + (size_t)row * 1024 + q * 128 + g * 32 + 16) = o1;
  TS_END(4, 3, ws);
}

extern "C" void kernel_launch(void* const* d_in, const int* in_sizes, int n_in, void* d_out, int out_size,
                              void* d_ws, size_t ws_size, hipStream_t stream) {
  const char* x = (const char*)d_in[0];
  const char* wqkv = (const char*)d_in[1];
  const char* wo = (const char*)d_in[2];
  const char* w1 = (const char*)d_in[4];
  const char* b1 = (const char*)d_in[5];
  const char* w2 = (const char*)d_in[6];
  const char* b2 = (const char*)d_in[7];
  char* ws = (char*)d_ws;
  k1_qkv<<<256, 512, 0, stream>>>(x, wqkv, wo, w1, w2, b1, ws);
  kr_prep<<<32, 64, 0, stream>>>(ws);
  k2_attn<<<256, 256, 0, stream>>>(x, ws);
  k3_up<<<256, 256, 0, stream>>>(b1, ws);
  k4_down<<<256, 256, 0, stream>>>(b2, ws, (char*)d_out);
}
```

```cpp
#include <hip/hip_runtime.h>
#include <hip/hip_bf16.h>
#include <stdint.h>

typedef __attribute__((ext_vector_type(8))) short bf16x8;
typedef __attribute__((ext_vector_type(4))) short bf16x4;
typedef __attribute__((ext_vector_type(4))) short v4i16_t;
typedef __attribute__((ext_vector_type(4))) float f32x4;
typedef __attribute__((ext_vector_type(2))) float f32x2;
typedef __attribute__((ext_vector_type(4))) unsigned u32x4;
typedef __attribute__((ext_vector_type(2))) unsigned u32x2;
typedef __attribute__((ext_vector_type(2))) __bf16 bfv2;

#define DEV __device__ __forceinline__
#define LDS_AS __attribute__((address_space(3)))

static constexpr size_t WS_PQ  = 0;
static constexpr size_t WS_H   = WS_PQ + (2u << 20);
static constexpr size_t WS_HM  = WS_H + (2u << 20);
static constexpr size_t WS_WO  = WS_HM + (7u << 20);
static constexpr size_t WS_W1  = WS_WO + (512u << 10);
static constexpr size_t WS_W2  = WS_W1 + (1792u << 10);
static constexpr size_t WS_KVP = WS_W2 + (1792u << 10);
static constexpr size_t WS_ZP  = WS_KVP + (512u << 10);
static constexpr size_t WS_KVF = WS_ZP + (32u << 10);
static constexpr size_t WS_ZF  = WS_KVF + (16u << 10);
static constexpr size_t WS_CS1 = WS_ZF + (16u << 10);
static constexpr size_t WS_HST = WS_CS1 + (24u << 10);

DEV f32x4 mfma32(bf16x8 a, bf16x8 b, f32x4 c) { return __builtin_amdgcn_mfma_f32_16x16x32_bf16(a, b, c, 0, 0, 0); }
DEV f32x4 mfma16(bf16x4 a, bf16x4 b, f32x4 c) { return __builtin_amdgcn_mfma_f32_16x16x16bf16_1k(a, b, c, 0, 0, 0); }

DEV unsigned pk2(float a, float b) {
  f32x2 v = {a, b};
  bfv2 r = __builtin_convertvector(v, bfv2);
  return __builtin_bit_cast(unsigned, r);
}
DEV float bflo(unsigned u) { return __builtin_bit_cast(float, u << 16); }
DEV float bfhi(unsigned u) { return __builtin_bit_cast(float, u & 0xffff0000u); }
DEV float bfr(float a) { return bflo(pk2(a, a)); }

DEV u32x4 ldg16(const void* p) { return *(const u32x4*)p; }
DEV u32x2 ldg8(const void* p) { return *(const u32x2*)p; }
DEV bf16x8 as_bf8(u32x4 v) { return __builtin_bit_cast(bf16x8, v); }
DEV bf16x4 as_bf4(u32x2 v) { return __builtin_bit_cast(bf16x4, v); }

DEV float phi_f(float t) {
  float e = __builtin_amdgcn_exp2f(t * 1.44269504088896341f);
  return (t > 0.f ? t + 1.f : e) + 1e-6f;
}

#define WAIT_V(n) asm volatile("s_waitcnt vmcnt(" #n ")" ::: "memory")
#define WAIT_L0() asm volatile("s_waitcnt lgkmcnt(0)" ::: "memory")
#define BAR() __builtin_amdgcn_s_barrier()

#define TS_DECL
#define TS(k)
#define TS_END(kid, n, wsp)
template <int N> DEV void wait_vm() { asm volatile("s_waitcnt vmcnt(%0)" ::"n"(N) : "memory"); }

DEV void ln_scalars(float mean, float var, float& a, float& b) {
  float mb = bfr(mean);
  float d = bfr(__builtin_sqrtf(bfr(bfr(var) + 1e-5f)));
  a = 1.0f / d;
  b = mb * a;
}

static constexpr int K1_WOFF = 4 * 16384;
static constexpr int K1_ST = K1_WOFF + 4 * 12288;
static constexpr int K1_CS = K1_ST + 1024;
static constexpr int K1_KV = K1_CS + 2048;
static constexpr int K1_Z = K1_KV + 8192;
static constexpr int K1_LDS = K1_Z + 512;

DEV void glds16(unsigned voff, const char* sbase, unsigned ldsdst) {
  unsigned keep;
  asm volatile("s_mov_b32 %0, m0\n\ts_mov_b32 m0, %3\n\ts_nop 0\n\tglobal_load_lds_dwordx4 %1, %2\n\ts_mov_b32 m0, %0"
               : "=&s"(keep) : "v"(voff), "s"(sbase), "s"(ldsdst) : "memory");
}
DEV void gld16(u32x4& d, unsigned voff, const char* sbase) {
  asm volatile("global_load_dwordx4 %0, %1, %2" : "=v"(d) : "v"(voff), "s"(sbase) : "memory");
}
DEV void gld8(u32x2& d, unsigned voff, const char* sbase) {
  asm volatile("global_load_dwordx2 %0, %1, %2" : "=v"(d) : "v"(voff), "s"(sbase) : "memory");
}
#define SB0() __builtin_amdgcn_sched_barrier(0)
#define MEMF() asm volatile("" ::: "memory")
DEV unsigned perm_lo(unsigned a, unsigned b) { return __builtin_amdgcn_perm(b, a, 0x05040100u); }
DEV unsigned perm_hi(unsigned a, unsigned b) { return __builtin_amdgcn_perm(b, a, 0x07060302u); }
DEV bf16x8 lds_b128(const char* lds, int off) { return *(const bf16x8*)(lds + off); }
DEV bf16x8 tr_frag(const char* lds, int off, int hi) {
  v4i16_t a = __builtin_amdgcn_ds_read_tr16_b64_v4i16((LDS_AS v4i16_t*)(lds + off));
  v4i16_t b = __builtin_amdgcn_ds_read_tr16_b64_v4i16((LDS_AS v4i16_t*)(lds + off + hi));
  return __builtin_shufflevector(a, b, 0, 1, 2, 3, 4, 5, 6, 7);
}
DEV float phi_p(float tp) {
  const float u = __builtin_fmaf(tp, 1.44269504088896341f, -1.44269504088896341e-6f);
  const float e = __builtin_amdgcn_fmed3f(__builtin_amdgcn_exp2f(u), 0.f, 1.f);
  return fmaxf(tp, 1e-6f) + e;
}
DEV void stat_acc(u32x4 v, float& s1, float& s2) {
#pragma unroll
  for (int d = 0; d < 4; ++d) {
    const float a = bflo(v[d]), c = bfhi(v[d]);
    s1 += a; s2 = __builtin_fmaf(a, a, s2);
    s1 += c; s2 = __builtin_fmaf(c, c, s2);
  }
}

__global__ __launch_bounds__(768, 3) void k1_qkv(const char* __restrict__ x, const char* __restrict__ wqkv,
                                                  const char* __restrict__ wo, const char* __restrict__ w1,
                                                  const char* __restrict__ w2, char* __restrict__ ws) {
  __shared__ __attribute__((aligned(16))) char lds[K1_LDS];
  TS_DECL
  const int tid = threadIdx.x, l = tid & 63, i = l & 15, g = l >> 4;
  const int w = __builtin_amdgcn_readfirstlane(tid >> 6);
  const int b = blockIdx.x, xcd = b & 7, idx = b >> 3;
  const int rt = (xcd >> 1) * 4 + (idx >> 3), hp = (xcd & 1) * 8 + (idx & 7);
  const f32x4 z4 = {0.f, 0.f, 0.f, 0.f};

  if (w >= 8) {
    const int lw = w - 8;
    const int rot = ((idx & 7) + 2 * (idx >> 3)) & 7;
    const unsigned lds0 = (unsigned)(uintptr_t)(LDS_AS char*)lds;
    unsigned vx[4], vw[3];
#pragma unroll
    for (int jj = 0; jj < 4; ++jj)
      vx[jj] = (32 * lw + 8 * jj + (l >> 3)) * 1024 + (((l & 7) ^ ((jj & 1) * 4 + (l >> 4))) * 16);
#pragma unroll
    for (int jj = 0; jj < 3; ++jj) {
      const int n = 64 * (3 * lw + jj) + l, row = n / 12, c = n - 12 * row;
      vw[jj] = row * 3072 + (c >> 2) * 1024 + ((((c >> 1) & 1) ^ ((row >> 3) & 1)) * 32) + (c & 1) * 16;
    }
    const char* xb = x + (size_t)rt * 131072;
    const char* wb = wqkv + hp * 64;
    const unsigned ldx = lds0 + lw * 4096, ldw = lds0 + K1_WOFF + lw * 3072;
#define K1_GX(S, JJ) glds16(vx[JJ], xb + (((S) + rot) & 7) * 128, ldx + ((S) & 3) * 16384 + (JJ) * 1024)
#define K1_GW(S, JJ) glds16(vw[JJ], wb + (size_t)(((S) + rot) & 7) * 196608, ldw + ((S) & 3) * 12288 + (JJ) * 1024)
#pragma unroll
    for (int s = 0; s < 2; ++s) {
      K1_GX(s, 0); K1_GW(s, 0); K1_GX(s, 1); K1_GW(s, 1); K1_GX(s, 2); K1_GW(s, 2); K1_GX(s, 3);
    }
    const int u = b * 4 + lw;
    const char* ssrc;
    char* sdst;
    int gstr, tstr, rstr, estr;
    if (u < 448) {
      const int uq = u >> 4, s = u & 15;
      ssrc = w1 + (size_t)(64 * (s >> 1) + 2 * (s & 1)) * 3584 + uq * 128;
      gstr = 16 * 3584; tstr = 3584; rstr = 4 * 3584; estr = 16384;
      sdst = ws + WS_W1 + (size_t)(uq * 64 + s) * 1024;
    } else if (u < 576) {
      const int uq = (u - 448) >> 4, s = u & 15;
      ssrc = wo + (size_t)(32 * s) * 1024 + uq * 128;
      gstr = 4096; tstr = 16384; rstr = 1024; estr = 1024;
      sdst = ws + WS_WO + (size_t)(uq * 16 + s) * 4096;
    } else {
      const int v = u - 576, uq = v / 56, s = v - uq * 56;
      ssrc = w2 + (size_t)(64 * (s >> 1) + 2 * (s & 1)) * 1024 + uq * 128;
      gstr = 16384; tstr = 1024; rstr = 4096; estr = 1024;
      sdst = ws + WS_W2 + (size_t)(uq * 56 + s) * 4096;
    }
    const unsigned soff = g * gstr + i * 8;
    u32x2 v8[8];
#pragma unroll
    for (int jj = 0; jj < 8; ++jj) gld8(v8[jj], soff, ssrc + (jj >> 2) * tstr + (jj & 3) * rstr);
    TS(1);
    int sa[4];
#pragma unroll
    for (int j = 0; j < 4; ++j) sa[j] = (32 * lw + (l >> 1)) * 128 + (l & 1) * 64 + ((j ^ ((l >> 2) & 3)) * 16);
    float s1 = 0.f, s2 = 0.f;
#pragma unroll
    for (int s = 0; s < 8; ++s) {
      if (s < 2) wait_vm<15>(); else if (s < 7) wait_vm<7>(); else wait_vm<0>();
      BAR();
      MEMF();
      if (s == 0) TS(2);
      if (s == 4) TS(3);
      SB0();
      u32x4 t[4];
#pragma unroll
      for (int j = 0; j < 4; ++j) t[j] = *(const u32x4*)(lds + sa[j] + (s & 3) * 16384);
      SB0();
      if (s < 6) { K1_GX(s + 2, 0); K1_GW(s + 2, 0); }
      SB0();
      stat_acc(t[0], s1, s2);
      SB0();
      if (s < 6) { K1_GX(s + 2, 1); K1_GW(s + 2, 1); }
      SB0();
      stat_acc(t[1], s1, s2);
      SB0();
      if (s < 6) { K1_GX(s + 2, 2); K1_GW(s + 2, 2); }
      SB0();
      stat_acc(t[2], s1, s2);
      SB0();
      if (s < 6) { K1_GX(s + 2, 3); }
      SB0();
      stat_acc(t[3], s1, s2);
      SB0();
    }
    TS(4);
    s1 += __builtin_bit_cast(float, __builtin_amdgcn_mov_dpp(__builtin_bit_cast(int, s1), 0xB1, 0xf, 0xf, true));
    s2 += __builtin_bit_cast(float, __builtin_amdgcn_mov_dpp(__builtin_bit_cast(int, s2), 0xB1, 0xf, 0xf, true));
    const float mean = s1 * (1.0f / 512.0f);
    const float var = fmaxf(s2 * (1.0f / 512.0f) - mean * mean, 0.f);
    const float rs = __builtin_amdgcn_rsqf(var + 1e-5f);
    if ((l & 1) == 0) *(f32x2*)(lds + K1_ST + (32 * lw + (l >> 1)) * 8) = (f32x2){rs, mean * rs};
    WAIT_L0();
    BAR();
    MEMF();
    asm volatile("" : "+v"(v8[0]), "+v"(v8[1]), "+v"(v8[2]), "+v"(v8[3]), "+v"(v8[4]), "+v"(v8[5]), "+v"(v8[6]), "+v"(v8[7]));
#pragma unroll
    for (int e = 0; e < 4; ++e) {
      u32x4 fo;
#pragma unroll
      for (int jj = 0; jj < 4; ++jj) {
        const unsigned a = v8[2 * jj][e >> 1], c = v8[2 * jj + 1][e >> 1];
        fo[jj] = (e & 1) ? perm_hi(a, c) : perm_lo(a, c);
      }
      *(u32x4*)(sdst + e * estr + l * 16) = fo;
    }
    TS_END(0, 5, ws);
    return;
  }

  const int rg = w & 3, h = w >> 2, kf = rg & 1;
  const u32x4 onesu = {0x3f803f80u, 0x3f803f80u, 0x3f803f80u, 0x3f803f80u};
  const bf16x8 ones = as_bf8(onesu);
  int xa[2][2], wa[2];
  {
    const int xr = (32 * rg + 16 * h + i) * 128 + ((g ^ ((i >> 1) & 7)) * 16);
    xa[0][0] = xr ^ (kf * 64); xa[0][1] = xa[0][0] ^ 2048;
    xa[1][0] = xa[0][0] ^ 64;  xa[1][1] = xa[1][0] ^ 2048;
    const int wr = K1_WOFF + (8 * g + (i >> 2)) * 192 + ((h ^ (g & 1)) * 32) + (i & 3) * 8;
    wa[0] = wr + kf * 6144; wa[1] = wr + (kf ^ 1) * 6144;
  }
  f32x4 aq[2] = {z4, z4}, ak[2] = {z4, z4}, av[2] = {z4, z4}, cq = z4, ck = z4, cv = z4;
  bf16x8 fx[2][2], fw[2][3];
#define K1_HALF(C, RD, SLOT, CS)                                                                               \
  SB0();                                                                                                       \
  if (RD) fx[(C) ^ 1][0] = lds_b128(lds, xa[(C) ^ 1][0] + (SLOT) * 16384);                                     \
  SB0();                                                                                                       \
  aq[0] = mfma32(fw[C][0], fx[C][0], aq[0]);                                                                   \
  SB0();                                                                                                       \
  if (RD) fw[(C) ^ 1][0] = tr_frag(lds, wa[(C) ^ 1] + (SLOT) * 12288, 768);                                    \
  SB0();                                                                                                       \
  aq[1] = mfma32(fw[C][0], fx[C][1], aq[1]);                                                                   \
  SB0();                                                                                                       \
  if (RD) fx[(C) ^ 1][1] = lds_b128(lds, xa[(C) ^ 1][1] + (SLOT) * 16384);                                     \
  SB0();                                                                                                       \
  ak[0] = mfma32(fx[C][0], fw[C][1], ak[0]);                                                                   \
  SB0();                                                                                                       \
  if (RD) fw[(C) ^ 1][1] = tr_frag(lds, wa[(C) ^ 1] + (SLOT) * 12288 + 64, 768);                               \
  SB0();                                                                                                       \
  ak[1] = mfma32(fx[C][1], fw[C][1], ak[1]);                                                                   \
  SB0();                                                                                                       \
  if (RD) fw[(C) ^ 1][2] = tr_frag(lds, wa[(C) ^ 1] + (SLOT) * 12288 + 128, 768);                              \
  SB0();                                                                                                       \
  av[0] = mfma32(fx[C][0], fw[C][2], av[0]);                                                                   \
  av[1] = mfma32(fx[C][1], fw[C][2], av[1]);                                                                   \
  if (CS) {                                                                                                    \
    cq = mfma32(fw[C][0], ones, cq);                                                                           \
    ck = mfma32(ones, fw[C][1], ck);                                                                           \
    cv = mfma32(ones, fw[C][2], cv);                                                                           \
  }                                                                                                            \
  SB0();
  TS(1);
  BAR();
  MEMF();
  TS(2);
  fx[0][0] = lds_b128(lds, xa[0][0]);
  fw[0][0] = tr_frag(lds, wa[0], 768);
  fx[0][1] = lds_b128(lds, xa[0][1]);
  fw[0][1] = tr_frag(lds, wa[0] + 64, 768);
  fw[0][2] = tr_frag(lds, wa[0] + 128, 768);
#pragma unroll
  for (int s = 0; s < 8; ++s) {
    K1_HALF(0, true, s & 3, true);
    if (s < 7) { BAR(); MEMF(); }
    if (s == 3) TS(3);
    K1_HALF(1, s < 7, (s + 1) & 3, false);
  }
  TS(4);
  if (i == 0) *(f32x4*)(lds + K1_CS + w * 192 + g * 16) = cq;
  if (g == 0) {
    *(float*)(lds + K1_CS + w * 192 + 64 + i * 4) = ck[0];
    *(float*)(lds + K1_CS + w * 192 + 128 + i * 4) = cv[0];
  }
  WAIT_L0();
  BAR();
  MEMF();
  cq += *(const f32x4*)(lds + K1_CS + (w ^ 1) * 192 + g * 16);
  const float csk = ck[0] + *(const float*)(lds + K1_CS + (w ^ 1) * 192 + 64 + i * 4);
  const float csv = cv[0] + *(const float*)(lds + K1_CS + (w ^ 1) * 192 + 128 + i * 4);
  u32x4 pq, pk, pv;
#pragma unroll
  for (int m = 0; m < 2; ++m) {
    const int rb = K1_ST + (32 * rg + 16 * (h ^ m)) * 8;
    const f32x2 sq = *(const f32x2*)(lds + rb + i * 8);
    const f32x4 s01 = *(const f32x4*)(lds + rb + g * 32);
    const f32x4 s23 = *(const f32x4*)(lds + rb + g * 32 + 16);
    float q[4], k[4], v[4];
#pragma unroll
    for (int r = 0; r < 4; ++r) {
      const float rs = r == 0 ? s01[0] : (r == 1 ? s01[2] : (r == 2 ? s23[0] : s23[2]));
      const float mr = r == 0 ? s01[1] : (r == 1 ? s01[3] : (r == 2 ? s23[1] : s23[3]));
      q[r] = phi_p(__builtin_fmaf(aq[m][r], sq[0], __builtin_fmaf(-sq[1], cq[r], 1e-6f)));
      k[r] = phi_p(__builtin_fmaf(ak[m][r], rs, __builtin_fmaf(-mr, csk, 1e-6f)));
      v[r] = __builtin_fmaf(av[m][r], rs, -mr * csv);
    }
    pq[2 * m] = pk2(q[0], q[1]); pq[2 * m + 1] = pk2(q[2], q[3]);
    pk[2 * m] = pk2(k[0], k[1]); pk[2 * m + 1] = pk2(k[2], k[3]);
    pv[2 * m] = pk2(v[0], v[1]); pv[2 * m + 1] = pk2(v[2], v[3]);
  }
  const f32x4 kvt = mfma32(as_bf8(pk), as_bf8(pv), z4);
  const f32x4 zz = mfma32(as_bf8(pk), ones, z4);
  *(u32x4*)(ws + WS_PQ + (size_t)((rt * 4 + rg) * 32 + 2 * hp + h) * 1024 + l * 16) = pq;
  *(f32x4*)(lds + K1_KV + w * 1024 + l * 16) = kvt;
  if (i == 0) *(f32x4*)(lds + K1_Z + w * 64 + g * 16) = zz;
  WAIT_L0();
  BAR();
  MEMF();
  if (w < 2) {
    f32x4 s = *(const f32x4*)(lds + K1_KV + w * 4096 + l * 16);
#pragma unroll
    for (int r = 1; r < 4; ++r) s += *(const f32x4*)(lds + K1_KV + w * 4096 + r * 1024 + l * 16);
    *(f32x4*)(ws + WS_KVP + (size_t)(rt * 32 + 2 * hp + w) * 1024 + l * 16) = s;
  } else if (w == 2 && l < 8) {
    f32x4 s = *(const f32x4*)(lds + K1_Z + (l >> 2) * 256 + (l & 3) * 16);
#pragma unroll
    for (int r = 1; r < 4; ++r) s += *(const f32x4*)(lds + K1_Z + (l >> 2) * 256 + r * 64 + (l & 3) * 16);
    *(f32x4*)(ws + WS_ZP + (size_t)(rt * 32 + 2 * hp) * 64 + l * 16) = s;
  }
  TS_END(0, 5, ws);
}

__global__ __launch_bounds__(64) void kr_prep(char* __restrict__ ws) {
  TS_DECL
  const int l = threadIdx.x, g = l >> 4;
  const int h = blockIdx.x;
  f32x4 s = {0.f, 0.f, 0.f, 0.f}, z = {0.f, 0.f, 0.f, 0.f};
  f32x4 t[16], tz[16];
#pragma unroll
  for (int r = 0; r < 16; ++r) {
    t[r] = *(const f32x4*)(ws + WS_KVP + (size_t)(r * 32 + h) * 1024 + l * 16);
    tz[r] = *(const f32x4*)(ws + WS_ZP + (size_t)(r * 32 + h) * 64 + g * 16);
  }
#pragma unroll
  for (int r = 0; r < 16; ++r) { s += t[r]; z += tz[r]; }
  *(u32x2*)(ws + WS_KVF + (h >> 1) * 1024 + l * 16 + (h & 1) * 8) = (u32x2){pk2(s[0], s[1]), pk2(s[2], s[3])};
  *(u32x2*)(ws + WS_ZF + (h >> 1) * 1024 + l * 16 + (h & 1) * 8) = (u32x2){pk2(z[0], z[1]), pk2(z[2], z[3])};
  TS_END(1, 1, ws);
}

__global__ __launch_bounds__(256, 1) void k2_attn(const char* __restrict__ x, char* __restrict__ ws) {
  __shared__ __attribute__((aligned(16))) char lds[4 * 3 * 4096];
  TS_DECL
  const int tid = threadIdx.x, l = tid & 63, i = l & 15, g = l >> 4;
  const int w = __builtin_amdgcn_readfirstlane(tid >> 6);
  const int b = blockIdx.x, xcd = b & 7, idx = b >> 3;
  const int rt = xcd * 4 + (idx >> 3), q = idx & 7;

  u32x4 kvf[4], zf[4], pqf[4][4], wf[4][4];
#pragma unroll
  for (int s = 0; s < 4; ++s) {
    const int sg = 4 * w + s;
    kvf[s] = ldg16(ws + WS_KVF + sg * 1024 + l * 16);
    zf[s] = ldg16(ws + WS_ZF + sg * 1024 + l * 16);
#pragma unroll
    for (int m = 0; m < 4; ++m)
      pqf[s][m] = ldg16(ws + WS_PQ + (size_t)((rt * 2 + (m >> 1)) * 32 + 2 * sg + (m & 1)) * 1024 + l * 16);
#pragma unroll
    for (int e = 0; e < 4; ++e) wf[s][e] = ldg16(ws + WS_WO + (size_t)((q * 16 + sg) * 4 + e) * 1024 + l * 16);
  }
  const int row = rt * 64 + w * 16 + i;
  const u32x4 xr0 = ldg16(x + (size_t)row * 1024 + q * 128 + g * 32);
  const u32x4 xr1 = ldg16(x + (size_t)row * 1024 + q * 128 + g * 32 + 16);

  f32x4 acc[4][4];
#pragma unroll
  for (int m = 0; m < 4; ++m)
#pragma unroll
    for (int e = 0; e < 4; ++e) acc[m][e] = (f32x4){0.f, 0.f, 0.f, 0.f};
  const f32x4 z4 = {0.f, 0.f, 0.f, 0.f};
#pragma unroll
  for (int s = 0; s < 4; ++s) {
#pragma unroll
    for (int m = 0; m < 4; ++m) {
      u32x4 of;
#pragma unroll
      for (int hh = 0; hh < 2; ++hh) {
        const u32x4 pt = pqf[s][(m & 2) + hh];
        const int po = 2 * ((m & 1) ^ hh);
        const bf16x4 pq = as_bf4((u32x2){pt[po], pt[po + 1]});
        const f32x4 num = mfma16(as_bf4((u32x2){kvf[s][2 * hh], kvf[s][2 * hh + 1]}), pq, z4);
        const f32x4 den = mfma16(as_bf4((u32x2){zf[s][2 * hh], zf[s][2 * hh + 1]}), pq, z4);
        const float rd = __builtin_amdgcn_rcpf(bfr(bfr(den[0]) + 1e-6f));
        of[2 * hh] = pk2(num[0] * rd, num[1] * rd);
        of[2 * hh + 1] = pk2(num[2] * rd, num[3] * rd);
      }
#pragma unroll
      for (int e = 0; e < 4; ++e) acc[m][e] = mfma32(as_bf8(wf[s][e]), as_bf8(of), acc[m][e]);
    }
  }
  TS(1);
#pragma unroll
  for (int m = 0; m < 4; ++m) {
    if (m != w) {
      const int slot = (w - m - 1) & 3;
#pragma unroll
      for (int e = 0; e < 4; ++e) *(f32x4*)(lds + (m * 3 + slot) * 4096 + e * 1024 + l * 16) = acc[m][e];
    }
  }
  __syncthreads();
  f32x4 fin[4];
#pragma unroll
  for (int e = 0; e < 4; ++e) fin[e] = w == 0 ? acc[0][e] : (w == 1 ? acc[1][e] : (w == 2 ? acc[2][e] : acc[3][e]));
#pragma unroll
  for (int sl = 0; sl < 3; ++sl)
#pragma unroll
    for (int e = 0; e < 4; ++e) fin[e] += *(const f32x4*)(lds + (w * 3 + sl) * 4096 + e * 1024 + l * 16);

  float hv[4][4];
  float sum = 0.f;
#pragma unroll
  for (int r = 0; r < 4; ++r) {
    const unsigned lo = r < 2 ? xr0[2 * r] : xr1[2 * r - 4];
    const unsigned hi = r < 2 ? xr0[2 * r + 1] : xr1[2 * r - 3];
    hv[0][r] = bfr(bflo(lo) + bfr(fin[0][r]));
    hv[1][r] = bfr(bfhi(lo) + bfr(fin[1][r]));
    hv[2][r] = bfr(bflo(hi) + bfr(fin[2][r]));
    hv[3][r] = bfr(bfhi(hi) + bfr(fin[3][r]));
    sum += (hv[0][r] + hv[1][r]) + (hv[2][r] + hv[3][r]);
  }
  const int mt = rt * 4 + w;
#pragma unroll
  for (int t = 0; t < 2; ++t) {
    u32x4 o = {pk2(hv[2 * t][0], hv[2 * t][1]), pk2(hv[2 * t][2], hv[2 * t][3]),
               pk2(hv[2 * t + 1][0], hv[2 * t + 1][1]), pk2(hv[2 * t + 1][2], hv[2 * t + 1][3])};
    *(u32x4*)(ws + WS_H + (size_t)(mt * 16 + 2 * q + t) * 1024 + l * 16) = o;
  }
  sum += __shfl_xor(sum, 16);
  sum += __shfl_xor(sum, 32);
  const float gm = sum * (1.0f / 64.0f);
  float m2 = 0.f;
#pragma unroll
  for (int e = 0; e < 4; ++e)
#pragma unroll
    for (int r = 0; r < 4; ++r) { const float d = hv[e][r] - gm; m2 += d * d; }
  m2 += __shfl_xor(m2, 16);
  m2 += __shfl_xor(m2, 32);
  if (g == 0) *(f32x2*)(ws + WS_HST + (size_t)row * 64 + q * 8) = (f32x2){gm, m2};
  TS_END(2, 2, ws);
}

static constexpr int K3_ST = 4 * 32768;
static constexpr int K3_CS = K3_ST + 1024;
static constexpr int K3_LDS = K3_CS + 512;

__global__ __launch_bounds__(768, 3) void k3_up(const char* __restrict__ b1, char* __restrict__ ws) {
  __shared__ __attribute__((aligned(16))) char lds[K3_LDS];
  TS_DECL
  const int tid = threadIdx.x, l = tid & 63, i = l & 15, g = l >> 4;
  const int w = __builtin_amdgcn_readfirstlane(tid >> 6);
  const int b = blockIdx.x, xcd = b & 7, idx = b >> 3;
  const int rhi = idx >= 14 ? 1 : 0;
  const int rt = xcd * 2 + rhi, cb = idx - 14 * rhi;
  const f32x4 z4 = {0.f, 0.f, 0.f, 0.f};

  if (w >= 8) {
    const int lw = w - 8;
    const int rot = (cb + 4 * rhi) & 7;
    const unsigned lds0 = (unsigned)(uintptr_t)(LDS_AS char*)lds;
    const unsigned v0 = l * 16, v1 = v0 + 1024, v2 = v0 + 16384, v3 = v0 + 17408;
    const char* xb = ws + WS_H + (size_t)(rt * 8 + 2 * lw) * 16384;
    const char* wb = ws + WS_W1 + (size_t)(cb * 8 + 2 * lw) * 16384;
    const unsigned ldx = lds0 + lw * 4096, ldw = lds0 + 16384 + lw * 4096;
    u32x4 hs0, hs1;
    {
      const char* sp = ws + WS_HST + (size_t)(rt * 128 + 32 * lw) * 64;
      gld16(hs0, l * 32, sp);
      gld16(hs1, l * 32 + 16, sp);
    }
#define K3_G(S, V, BASE, LD, JJ) glds16(V, BASE + (((S) + rot) & 7) * 2048, LD + ((S) & 3) * 32768 + (JJ) * 1024)
#define K3_G01(S) K3_G(S, v0, xb, ldx, 0); K3_G(S, v0, wb, ldw, 0); K3_G(S, v2, xb, ldx, 2); K3_G(S, v2, wb, ldw, 2)
#define K3_G23(S) K3_G(S, v1, xb, ldx, 1); K3_G(S, v1, wb, ldw, 1); K3_G(S, v3, xb, ldx, 3); K3_G(S, v3, wb, ldw, 3)
    K3_G01(0); K3_G23(0); K3_G01(1); K3_G23(1);
    TS(1);
    const u32x4 onesu = {0x3f803f80u, 0x3f803f80u, 0x3f803f80u, 0x3f803f80u};
    const bf16x8 ones = as_bf8(onesu);
    f32x4 c0 = z4, c1 = z4;
    const int ca = 16384 + lw * 4096 + l * 16;
#pragma unroll
    for (int s = 0; s < 8; ++s) {
      if (s < 7) wait_vm<8>(); else wait_vm<0>();
      BAR();
      MEMF();
      if (s == 0) TS(2);
      if (s == 4) TS(3);
      SB0();
      bf16x8 t[4];
#pragma unroll
      for (int j = 0; j < 4; ++j) t[j] = lds_b128(lds, ca + (s & 3) * 32768 + j * 1024);
      SB0();
      if (s < 6) { K3_G01(s + 2); }
      SB0();
      c0 = mfma32(t[0], ones, c0);
      c1 = mfma32(t[2], ones, c1);
      c0 = mfma32(t[1], ones, c0);
      c1 = mfma32(t[3], ones, c1);
      SB0();
      if (s < 6) { K3_G23(s + 2); }
      SB0();
      if (s == 0) {
        asm volatile("" : "+v"(hs0), "+v"(hs1));
        const f32x4 p0 = __builtin_bit_cast(f32x4, hs0), p1 = __builtin_bit_cast(f32x4, hs1);
        float sm = (p0[0] + p0[2]) + (p1[0] + p1[2]);
        sm += __builtin_bit_cast(float, __builtin_amdgcn_mov_dpp(__builtin_bit_cast(int, sm), 0xB1, 0xf, 0xf, true));
        const float mean = sm * 0.125f;
        const float d0 = p0[0] - mean, d1 = p0[2] - mean, d2 = p1[0] - mean, d3 = p1[2] - mean;
        float m2 = (p0[1] + p0[3]) + (p1[1] + p1[3]) + 64.f * ((d0 * d0 + d1 * d1) + (d2 * d2 + d3 * d3));
        m2 += __builtin_bit_cast(float, __builtin_amdgcn_mov_dpp(__builtin_bit_cast(int, m2), 0xB1, 0xf, 0xf, true));
        const float var = m2 * (1.0f / 512.0f);
        const float mb = bfr(mean);
        const float r1 = 1.0f / bfr(__builtin_sqrtf(bfr(bfr(var) + 1e-5f)));
        const float mn2 = bfr((mean - mb) * r1);
        const float var2 = var * r1 * r1;
        const float r2 = 1.0f / bfr(__builtin_sqrtf(bfr(bfr(var2) + 1e-5f)));
        if ((l & 1) == 0) *(f32x2*)(lds + K3_ST + (32 * lw + (l >> 1)) * 8) = (f32x2){r1 * r2, (mb * r1 + mn2) * r2};
        SB0();
      }
    }
    TS(4);
    if (i == 0) {
      *(f32x4*)(lds + K3_CS + lw * 128 + g * 16) = c0;
      *(f32x4*)(lds + K3_CS + lw * 128 + 64 + g * 16) = c1;
    }
    WAIT_L0();
    BAR();
    TS_END(3, 5, ws);
    return;
  }

  const int rg = w & 3, nh = w >> 2;
  const int uq = cb * 2 + nh;
  u32x4 bb0, bb1;
  gld16(bb0, g * 32, b1 + uq * 128);
  gld16(bb1, g * 32 + 16, b1 + uq * 128);
  const int xa = rg * 4096 + l * 16, wa = 16384 + nh * 8192 + l * 16;
  f32x4 acc[2][4];
#pragma unroll
  for (int m = 0; m < 2; ++m)
#pragma unroll
    for (int e = 0; e < 4; ++e) acc[m][e] = z4;
  bf16x8 fx[2][2], fw[2][4];
#define K3_HALF(C, RD, SLOT)                                                                                   \
  SB0();                                                                                                       \
  if (RD) fx[(C) ^ 1][0] = lds_b128(lds, xa + (SLOT) * 32768 + ((C) ^ 1) * 1024);                              \
  SB0();                                                                                                       \
  acc[0][0] = mfma32(fw[C][0], fx[C][0], acc[0][0]);                                                           \
  SB0();                                                                                                       \
  if (RD) fw[(C) ^ 1][0] = lds_b128(lds, wa + (SLOT) * 32768 + ((C) ^ 1) * 1024);                              \
  SB0();                                                                                                       \
  acc[1][0] = mfma32(fw[C][0], fx[C][1], acc[1][0]);                                                           \
  SB0();                                                                                                       \
  if (RD) fx[(C) ^ 1][1] = lds_b128(lds, xa + (SLOT) * 32768 + ((C) ^ 1) * 1024 + 2048);                       \
  SB0();                                                                                                       \
  acc[0][1] = mfma32(fw[C][1], fx[C][0], acc[0][1]);                                                           \
  SB0();                                                                                                       \
  if (RD) fw[(C) ^ 1][1] = lds_b128(lds, wa + (SLOT) * 32768 + ((C) ^ 1) * 1024 + 2048);                       \
  SB0();                                                                                                       \
  acc[1][1] = mfma32(fw[C][1], fx[C][1], acc[1][1]);                                                           \
  SB0();                                                                                                       \
  if (RD) fw[(C) ^ 1][2] = lds_b128(lds, wa + (SLOT) * 32768 + ((C) ^ 1) * 1024 + 4096);                       \
  SB0();                                                                                                       \
  acc[0][2] = mfma32(fw[C][2], fx[C][0], acc[0][2]);                                                           \
  SB0();                                                                                                       \
  if (RD) fw[(C) ^ 1][3] = lds_b128(lds, wa + (SLOT) * 32768 + ((C) ^ 1) * 1024 + 6144);                       \
  SB0();                                                                                                       \
  acc[1][2] = mfma32(fw[C][2], fx[C][1], acc[1][2]);                                                           \
  acc[0][3] = mfma32(fw[C][3], fx[C][0], acc[0][3]);                                                           \
  acc[1][3] = mfma32(fw[C][3], fx[C][1], acc[1][3]);                                                           \
  SB0();
  TS(1);
  BAR();
  MEMF();
  TS(2);
  fx[0][0] = lds_b128(lds, xa);
  fw[0][0] = lds_b128(lds, wa);
  fx[0][1] = lds_b128(lds, xa + 2048);
  fw[0][1] = lds_b128(lds, wa + 2048);
  fw[0][2] = lds_b128(lds, wa + 4096);
  fw[0][3] = lds_b128(lds, wa + 6144);
#pragma unroll
  for (int s = 0; s < 8; ++s) {
    K3_HALF(0, true, s & 3);
    if (s < 7) { BAR(); MEMF(); }
    if (s == 3) TS(3);
    K3_HALF(1, s < 7, (s + 1) & 3);
  }
  TS(4);
  BAR();
  MEMF();
  asm volatile("s_waitcnt vmcnt(0)" : "+v"(bb0), "+v"(bb1)::"memory");
  f32x4 cs[4];
#pragma unroll
  for (int e = 0; e < 4; ++e) cs[e] = *(const f32x4*)(lds + K3_CS + (nh * 4 + e) * 64 + g * 16);
#pragma unroll
  for (int m = 0; m < 2; ++m) {
    const f32x2 sc = *(const f32x2*)(lds + K3_ST + (32 * rg + 16 * m + i) * 8);
#pragma unroll
    for (int t = 0; t < 2; ++t) {
      u32x4 o;
#pragma unroll
      for (int e2 = 0; e2 < 2; ++e2) {
        const int e = 2 * t + e2;
        float gl[4];
#pragma unroll
        for (int r = 0; r < 4; ++r) {
          const unsigned bu = (r < 2 ? bb0 : bb1)[2 * (r & 1) + t];
          const float bv = e2 ? bfhi(bu) : bflo(bu);
          const float tt = __builtin_fmaf(acc[m][e][r], sc[0], __builtin_fmaf(-sc[1], cs[e][r], bv));
          const float u = tt * (tt * tt * (-0.044715f * 2.302208198f) - 2.302208198f);
          gl[r] = tt * __builtin_amdgcn_rcpf(1.0f + __builtin_amdgcn_exp2f(u));
        }
        o[2 * e2] = pk2(gl[0], gl[1]);
        o[2 * e2 + 1] = pk2(gl[2], gl[3]);
      }
      *(u32x4*)(ws + WS_HM + (size_t)((rt * 8 + rg * 2 + m) * 56 + 2 * uq + t) * 1024 + l * 16) = o;
    }
  }
  TS_END(3, 5, ws);
}

__global__ __launch_bounds__(256, 1) void k4_down(const char* __restrict__ b2, const char* __restrict__ ws,
                                                   char* __restrict__ out) {
  __shared__ __attribute__((aligned(16))) char lds[4 * 3 * 4096];
  TS_DECL
  const int tid = threadIdx.x, l = tid & 63, i = l & 15, g = l >> 4;
  const int w = __builtin_amdgcn_readfirstlane(tid >> 6);
  const int b = blockIdx.x, xcd = b & 7, idx = b >> 3;
  const int rt = xcd * 4 + (idx >> 3), q = idx & 7;

  const char* xsrc = ws + WS_HM + (size_t)((rt * 4) * 56 + 14 * w) * 1024 + l * 16;
  const char* wsrc = ws + WS_W2 + (size_t)((q * 56 + 14 * w) * 4) * 1024 + l * 16;
  u32x4 xf[7][4], wf[7][4];
#pragma unroll
  for (int s = 0; s < 7; ++s) {
#pragma unroll
    for (int m = 0; m < 4; ++m) xf[s][m] = ldg16(xsrc + (size_t)(m * 56 + s) * 1024);
#pragma unroll
    for (int e = 0; e < 4; ++e) wf[s][e] = ldg16(wsrc + (size_t)(s * 4 + e) * 1024);
  }
  f32x4 acc[4][4];
#pragma unroll
  for (int m = 0; m < 4; ++m)
#pragma unroll
    for (int e = 0; e < 4; ++e) acc[m][e] = (f32x4){0.f, 0.f, 0.f, 0.f};
#pragma unroll
  for (int s = 0; s < 7; ++s) {
#pragma unroll
    for (int m = 0; m < 4; ++m)
#pragma unroll
      for (int e = 0; e < 4; ++e) acc[m][e] = mfma32(as_bf8(wf[s][e]), as_bf8(xf[s][m]), acc[m][e]);
    __builtin_amdgcn_sched_barrier(0);
#pragma unroll
    for (int m = 0; m < 4; ++m) xf[s][m] = ldg16(xsrc + (size_t)(m * 56 + 7 + s) * 1024);
#pragma unroll
    for (int e = 0; e < 4; ++e) wf[s][e] = ldg16(wsrc + (size_t)((7 + s) * 4 + e) * 1024);
    __builtin_amdgcn_sched_barrier(0);
  }
  TS(1);
  const int mt = rt * 4 + w;
  const u32x4 h0 = ldg16(ws + WS_H + (size_t)(mt * 16 + 2 * q) * 1024 + l * 16);
  const u32x4 h1 = ldg16(ws + WS_H + (size_t)(mt * 16 + 2 * q + 1) * 1024 + l * 16);
  const u32x4 bb0 = ldg16(b2 + q * 128 + g * 32);
  const u32x4 bb1 = ldg16(b2 + q * 128 + g * 32 + 16);
#pragma unroll
  for (int s = 0; s < 7; ++s) {
#pragma unroll
    for (int m = 0; m < 4; ++m)
#pragma unroll
      for (int e = 0; e < 4; ++e) acc[m][e] = mfma32(as_bf8(wf[s][e]), as_bf8(xf[s][m]), acc[m][e]);
  }
  TS(2);
#pragma unroll
  for (int m = 0; m < 4; ++m) {
    if (m != w) {
      const int slot = (w - m - 1) & 3;
#pragma unroll
      for (int e = 0; e < 4; ++e) *(f32x4*)(lds + (m * 3 + slot) * 4096 + e * 1024 + l * 16) = acc[m][e];
    }
  }
  __syncthreads();
  f32x4 fin[4];
#pragma unroll
  for (int e = 0; e < 4; ++e) fin[e] = w == 0 ? acc[0][e] : (w == 1 ? acc[1][e] : (w == 2 ? acc[2][e] : acc[3][e]));
#pragma unroll
  for (int sl = 0; sl < 3; ++sl)
#pragma unroll
    for (int e = 0; e < 4; ++e) fin[e] += *(const f32x4*)(lds + (w * 3 + sl) * 4096 + e * 1024 + l * 16);

  float ov[4][4];
#pragma unroll
  for (int e = 0; e < 4; ++e) {
#pragma unroll
    for (int r = 0; r < 4; ++r) {
      const unsigned hu = (e < 2 ? h0 : h1)[2 * (e & 1) + (r >> 1)];
      const float hval = (r & 1) ? bfhi(hu) : bflo(hu);
      const unsigned bu = (r < 2 ? bb0 : bb1)[2 * (r & 1) + (e >> 1)];
      const float bval = (e & 1) ? bfhi(bu) : bflo(bu);
      ov[e][r] = hval + bfr(bfr(fin[e][r]) + bval);
    }
  }
  const int row = rt * 64 + w * 16 + i;
  u32x4 o0 = {pk2(ov[0][0], ov[1][0]), pk2(ov[2][0], ov[3][0]), pk2(ov[0][1], ov[1][1]), pk2(ov[2][1], ov[3][1])};
  u32x4 o1 = {pk2(ov[0][2], ov[1][2]), pk2(ov[2][2], ov[3][2]), pk2(ov[0][3], ov[1][3]), pk2(ov[2][3], ov[3][3])};
  *(u32x4*)(out + (size_t)row * 1024 + q * 128 + g * 32) = o0;
  *(u32x4*)(out + (size_t)row * 1024 + q * 128 + g * 32 + 16) = o1;
  TS_END(4, 3, ws);
}

extern "C" void kernel_launch(void* const* d_in, const int* in_sizes, int n_in, void* d_out, int out_size,
                              void* d_ws, size_t ws_size, hipStream_t stream) {
  const char* x = (const char*)d_in[0];
  const char* wqkv = (const char*)d_in[1];
  const char* wo = (const char*)d_in[2];
  const char* w1 = (const char*)d_in[4];
  const char* b1 = (const char*)d_in[5];
  const char* w2 = (const char*)d_in[6];
  const char* b2 = (const char*)d_in[7];
  char* ws = (char*)d_ws;
  k1_qkv<<<256, 768, 0, stream>>>(x, wqkv, wo, w1, w2, ws);
  kr_prep<<<32, 64, 0, stream>>>(ws);
  k2_attn<<<256, 256, 0, stream>>>(x, ws);
  k3_up<<<224, 768, 0, stream>>>(b1, ws);
  k4_down<<<256, 256, 0, stream>>>(b2, ws, (char*)d_out);
}
```

```cpp
#include <hip/hip_runtime.h>
#include <hip/hip_bf16.h>
#include <stdint.h>

typedef __attribute__((ext_vector_type(8))) short bf16x8;
typedef __attribute__((ext_vector_type(4))) short bf16x4;
typedef __attribute__((ext_vector_type(4))) short v4i16_t;
typedef __attribute__((ext_vector_type(4))) float f32x4;
typedef __attribute__((ext_vector_type(2))) float f32x2;
typedef __attribute__((ext_vector_type(4))) unsigned u32x4;
typedef __attribute__((ext_vector_type(2))) unsigned u32x2;
typedef __attribute__((ext_vector_type(2))) __bf16 bfv2;

#define DEV __device__ __forceinline__
#define LDS_AS __attribute__((address_space(3)))

static constexpr size_t WS_PQ  = 0;
static constexpr size_t WS_H   = WS_PQ + (2u << 20);
static constexpr size_t WS_HM  = WS_H + (2u << 20);
static constexpr size_t WS_KVP = WS_HM + (7u << 20);
static constexpr size_t WS_ZP  = WS_KVP + (512u << 10);
static constexpr size_t WS_KVF = WS_ZP + (32u << 10);
static constexpr size_t WS_ZF  = WS_KVF + (16u << 10);
static constexpr size_t WS_ST  = WS_KVF + (32u << 10);

#define W1K 1
#define WKR 3
#define WK2 11
#define K2_DLY 0
#define CPF 1
#define CODE_KB 32
#define K2_TRIES 2000
#define K2_B0 6
#define K2_B1 7
#define K2_PJ 2
#define K2_VG 2
#define WK3S 42
#define WK3T 4

DEV f32x4 mfma32(bf16x8 a, bf16x8 b, f32x4 c) { return __builtin_amdgcn_mfma_f32_16x16x32_bf16(a, b, c, 0, 0, 0); }
DEV f32x4 mfma16(bf16x4 a, bf16x4 b, f32x4 c) { return __builtin_amdgcn_mfma_f32_16x16x16bf16_1k(a, b, c, 0, 0, 0); }

DEV unsigned pk2(float a, float b) {
  f32x2 v = {a, b};
  bfv2 r = __builtin_convertvector(v, bfv2);
  return __builtin_bit_cast(unsigned, r);
}
DEV float bflo(unsigned u) { return __builtin_bit_cast(float, u << 16); }
DEV float bfhi(unsigned u) { return __builtin_bit_cast(float, u & 0xffff0000u); }
DEV float bfr(float a) { return bflo(pk2(a, a)); }

DEV u32x4 ldg16(const void* p) { return *(const u32x4*)p; }
DEV u32x2 ldg8(const void* p) { return *(const u32x2*)p; }
DEV bf16x8 as_bf8(u32x4 v) { return __builtin_bit_cast(bf16x8, v); }
DEV bf16x4 as_bf4(u32x2 v) { return __builtin_bit_cast(bf16x4, v); }

DEV float phi_f(float t) {
  float e = __builtin_amdgcn_exp2f(t * 1.44269504088896341f);
  return (t > 0.f ? t + 1.f : e) + 1e-6f;
}

#define WAIT_V(n) asm volatile("s_waitcnt vmcnt(" #n ")" ::: "memory")
#define WAIT_L0() asm volatile("s_waitcnt lgkmcnt(0)" ::: "memory")
#define BAR() __builtin_amdgcn_s_barrier()

#define TS_DECL
#define TS(k)
#define TS_END(kid, n, wsp)
template <int N> DEV void wait_vm() { asm volatile("s_waitcnt vmcnt(%0)" ::"n"(N) : "memory"); }

DEV void ln_scalars(float mean, float var, float& a, float& b) {
  float mb = bfr(mean);
  float d = bfr(__builtin_sqrtf(bfr(bfr(var) + 1e-5f)));
  a = 1.0f / d;
  b = mb * a;
}

static constexpr int K1_WOFF = 4 * 16384;
static constexpr int K1_ST = K1_WOFF + 4 * 12288;
static constexpr int K1_CS = K1_ST + 1024;
static constexpr int K1_KV = K1_CS + 2048;
static constexpr int K1_Z = K1_KV + 8192;
static constexpr int K1_LDS = K1_Z + 512;

DEV void glds16(unsigned voff, const char* sbase, unsigned ldsdst) {
  unsigned keep;
  asm volatile("s_mov_b32 %0, m0\n\ts_mov_b32 m0, %3\n\ts_nop 0\n\tglobal_load_lds_dwordx4 %1, %2\n\ts_mov_b32 m0, %0"
               : "=&s"(keep) : "v"(voff), "s"(sbase), "s"(ldsdst) : "memory");
}
DEV void gld16(u32x4& d, unsigned voff, const char* sbase) {
  asm volatile("global_load_dwordx4 %0, %1, %2" : "=v"(d) : "v"(voff), "s"(sbase) : "memory");
}
DEV void gld8(u32x2& d, unsigned voff, const char* sbase) {
  asm volatile("global_load_dwordx2 %0, %1, %2" : "=v"(d) : "v"(voff), "s"(sbase) : "memory");
}
#define ST_POL 3
#define ST_SFX " sc0 sc1"
DEV void gst16(char* p, u32x4 v) {
  asm volatile("global_store_dwordx4 %0, %1, off" ST_SFX ::"v"(p), "v"(v) : "memory");
}
DEV void gst8(char* p, u32x2 v) {
  asm volatile("global_store_dwordx2 %0, %1, off" ST_SFX ::"v"(p), "v"(v) : "memory");
}
#define SB0() __builtin_amdgcn_sched_barrier(0)
#define MEMF() asm volatile("" ::: "memory")
DEV unsigned perm_lo(unsigned a, unsigned b) { return __builtin_amdgcn_perm(b, a, 0x05040100u); }
DEV unsigned perm_hi(unsigned a, unsigned b) { return __builtin_amdgcn_perm(b, a, 0x07060302u); }
DEV bf16x8 lds_b128(const char* lds, int off) { return *(const bf16x8*)(lds + off); }
DEV bf16x8 tr_frag(const char* lds, int off, int hi) {
  v4i16_t a = __builtin_amdgcn_ds_read_tr16_b64_v4i16((LDS_AS v4i16_t*)(lds + off));
  v4i16_t b = __builtin_amdgcn_ds_read_tr16_b64_v4i16((LDS_AS v4i16_t*)(lds + off + hi));
  return __builtin_shufflevector(a, b, 0, 1, 2, 3, 4, 5, 6, 7);
}
DEV float phi_p(float tp) {
  const float u = __builtin_fmaf(tp, 1.44269504088896341f, -1.44269504088896341e-6f);
  const float e = __builtin_amdgcn_fmed3f(__builtin_amdgcn_exp2f(u), 0.f, 1.f);
  return fmaxf(tp, 1e-6f) + e;
}
DEV void stat_acc(u32x4 v, float& s1, float& s2) {
  const bfv2 one = __builtin_bit_cast(bfv2, 0x3f803f80u);
#pragma unroll
  for (int d = 0; d < 4; ++d) {
    const unsigned au = v[d];
    const bfv2 a = __builtin_bit_cast(bfv2, au);
    s1 = __builtin_amdgcn_fdot2_f32_bf16(a, one, s1, false);
    s2 = __builtin_amdgcn_fdot2_f32_bf16(a, a, s2, false);
  }
}

static constexpr int K2_KV = 8 * 16384;
static constexpr int K2_Z = K2_KV + 16384;
static constexpr int K2_SCR = K2_Z + 1024;
static constexpr int K2_LDS = K2_SCR + 4096;

DEV unsigned not_ones(unsigned v) { return v == 0xffffffffu ? 0x7fc07fc0u : v; }
DEV u32x2 red_slow(const char* p, int stride) {
  f32x4 s = {0.f, 0.f, 0.f, 0.f};
#pragma unroll 1
  for (int r = 0; r < 16; ++r) s += *(const f32x4*)(p + r * stride);
  return (u32x2){pk2(s[0], s[1]), pk2(s[2], s[3])};
}

extern "C" __global__ __launch_bounds__(768, 3) void k2_attn(const char* __restrict__ x, const char* __restrict__ wo,
                                                   const char* __restrict__ w1, char* __restrict__ ws) {
  __shared__ __attribute__((aligned(16))) char lds[K2_LDS];
  TS_DECL
  asm volatile("" ::"s"(x), "s"(wo), "s"(w1), "s"(ws));
  const int tid = threadIdx.x, l = tid & 63, i = l & 15, g = l >> 4;
  const int w = __builtin_amdgcn_readfirstlane(tid >> 6);
  const int b = blockIdx.x, xcd = b & 7, idx = b >> 3;
  const int rt = xcd * 4 + (idx >> 3), q = idx & 7;
  const int rot = 2 * (((idx >> 3) + q) & 3);
  const f32x4 z4 = {0.f, 0.f, 0.f, 0.f};

  if (w >= 8) {
    const int lw = w - 8;
    const unsigned lds0 = (unsigned)(uintptr_t)(LDS_AS char*)lds;
    const unsigned v0 = l * 16;
    const unsigned vw = (l >> 3) * 1024 + ((((l >> 1) & 3) ^ ((l >> 4) & 3)) * 32) + (l & 1) * 16;
    const char* pb = ws + WS_PQ + (size_t)(rt * 4 + lw) * 16384;
    const char* wb = wo + q * 128 + lw * 16384;
    const unsigned ld = lds0 + lw * 2048;
#pragma unroll
    for (int s = 0; s < 8; ++s) {
      const int sa_ = (s + rot) & 7;
      if (s == K2_B0) { wait_vm<4 * K2_B0 - 8>(); BAR(); }
      if (s == K2_B1) { wait_vm<4 * K2_B1 - 16>(); BAR(); }
      glds16(v0, pb + sa_ * 2048, ld + s * 16384);
      glds16(vw, wb + sa_ * 65536, ld + s * 16384 + 8192);
      glds16(v0, pb + sa_ * 2048 + 1024, ld + s * 16384 + 1024);
      glds16(vw, wb + sa_ * 65536 + 8192, ld + s * 16384 + 8192 + 1024);
    }
    const char* pw = w1 + (idx * 4 + lw) * 14336;
#pragma unroll
    for (int j = 0; j < WKR; ++j) glds16(v0, pw + j * 1024, lds0 + K2_SCR + lw * 1024);
    TS(1);
    TS(2);
    if (K2_B0 > 7) { wait_vm<24 + WKR>(); BAR(); }
    if (K2_B1 > 7) { wait_vm<16 + WKR>(); BAR(); }
    wait_vm<8 + WKR>(); BAR();
    wait_vm<WKR>();
    TS(3);
    BAR();
    BAR();
    MEMF();
    TS(4);
#pragma unroll
    for (int j = 14 - WK2; j < 14; ++j) glds16(v0, pw + j * 1024, lds0 + K2_SCR + lw * 1024);
    BAR();
    wait_vm<0>();
    TS_END(2, 5, ws);
    return;
  }

  const int mp = w & 1, kq = w >> 1, kh = kq & 1, mo = 2 * mp + (kq >> 1);
  if (b < 160 && w == 0) {
    const bool isk = b < 128;
    const int hd = isk ? b >> 2 : b - 128;
    const char* pp = isk ? ws + WS_KVP + hd * 1024 + (b & 3) * 256 : ws + WS_ZP + hd * 64;
    const unsigned pv = isk ? g * 32768 + i * 16 : g * 2048 + (i & 3) * 16;
    const int ps = isk ? 131072 : 8192;
    u32x4 t[4];
#pragma unroll
    for (int r = 0; r < 4; ++r) gld16(t[r], pv, pp + r * ps);
    asm volatile("s_waitcnt vmcnt(0)" : "+v"(t[0]), "+v"(t[1]), "+v"(t[2]), "+v"(t[3])::"memory");
    f32x4 sm = (__builtin_bit_cast(f32x4, t[0]) + __builtin_bit_cast(f32x4, t[1])) +
               (__builtin_bit_cast(f32x4, t[2]) + __builtin_bit_cast(f32x4, t[3]));
    float sv[4];
#pragma unroll
    for (int r = 0; r < 4; ++r) {
      const float f = sm[r];
      auto p1 = __builtin_amdgcn_permlane16_swap(__builtin_bit_cast(unsigned, f), __builtin_bit_cast(unsigned, f), false, false);
      const unsigned p10 = p1[0], p11 = p1[1];
      const float f2 = __builtin_bit_cast(float, p10) + __builtin_bit_cast(float, p11);
      auto p2 = __builtin_amdgcn_permlane32_swap(__builtin_bit_cast(unsigned, f2), __builtin_bit_cast(unsigned, f2), false, false);
      const unsigned p20 = p2[0], p21 = p2[1];
      sv[r] = __builtin_bit_cast(float, p20) + __builtin_bit_cast(float, p21);
    }
    const u32x2 pv2 = {not_ones(pk2(sv[0], sv[1])), not_ones(pk2(sv[2], sv[3]))};
    char* pd = ws + (isk ? WS_KVF + (hd >> 1) * 1024 + (b & 3) * 256 : WS_ZF + (hd >> 1) * 64) + (hd & 1) * 8 + l * 16;
    if (l < (isk ? 16 : 4)) asm volatile("global_store_dwordx2 %0, %1, off sc1" ::"v"(pd), "v"(pv2) : "memory");
  }
  u32x2 xr0, xr1;
  {
    const char* xp = x + (size_t)(rt * 64 + mo * 16) * 1024 + q * 128 + kh * 64;
    gld8(xr0, i * 1024 + g * 8, xp);
    gld8(xr1, i * 1024 + g * 8 + 32, xp);
  }
  const int sb = (kq >> 1) * 16384;
  const int pa0 = sb + (mo * 2 + kh) * 1024 + l * 16, pa1 = pa0 ^ 2048;
  const int kva = K2_KV + kq * 1024 + l * 16, za = K2_Z + kq * 64 + g * 16;
  int wa[4];
  {
    const int wr = sb + 8192 + (kh * 32 + 4 * g + (i >> 2)) * 128 + (i & 3) * 8;
    const int f = (2 * g + (i >> 3)) & 3;
#pragma unroll
    for (int e = 0; e < 4; ++e) wa[e] = wr + ((e ^ (2 * kh) ^ f) * 32);
  }
  const f32x4 eps4 = {1e-6f, 1e-6f, 1e-6f, 1e-6f};
  f32x4 acc[2][4] = {{z4, z4, z4, z4}, {z4, z4, z4, z4}};
  bf16x8 fw[4][4];
  u32x4 pq[4];
  u32x4 kv[2], zf[2];
  f32x4 dd[2], n0[2], n1[2];
  const unsigned mza = (i & 3) == 0 ? ~0u : 0u, mzb = (i & 3) == 1 ? ~0u : 0u;
#define K2_RK(J) do { kv[(J) & 1] = *(const u32x4*)(lds + kva + (J) * 4096); zf[(J) & 1] = *(const u32x4*)(lds + za + (J) * 256); } while (0)
#define K2_ND(U)                                                                                               \
  do {                                                                                                         \
    const u32x4 zq_ = zf[((U) >> 1) & 1], kq_ = kv[((U) >> 1) & 1];                                            \
    dd[(U) & 1] = mfma32(as_bf8((u32x4){zq_[0] & mza, zq_[1] & mza, zq_[2] & mzb, zq_[3] & mzb}), as_bf8(pq[(U) & 3]), eps4); \
    n0[(U) & 1] = mfma32(as_bf8((u32x4){kq_[0], kq_[1], 0u, 0u}), as_bf8(pq[(U) & 3]), z4);                     \
    n1[(U) & 1] = mfma32(as_bf8((u32x4){0u, 0u, kq_[2], kq_[3]}), as_bf8(pq[(U) & 3]), z4);                     \
  } while (0)
  const int sa = (w + rot) & 7;
  const char* kp = ws + WS_KVF + sa * 2048;
  const char* zp = ws + WS_ZF + sa * 128;
  u32x4 ka, kc, zc;
#define K2_POLL()                                                                                              \
  asm volatile("global_load_dwordx4 %0, %3, %4 sc1\n\tglobal_load_dwordx4 %1, %3, %4 offset:1024 sc1\n\t"       \
               "global_load_dwordx4 %2, %5, %6 sc1"                                                            \
               : "=&v"(ka), "=&v"(kc), "=&v"(zc) : "v"(l * 16), "s"(kp), "v"((l & 7) * 16), "s"(zp) : "memory")
  TS(1);
#pragma unroll
  for (int j = 0; j < 4; ++j) {
    BAR();
    MEMF();
    if (j == K2_PJ) K2_POLL();
#pragma unroll
    for (int e = 0; e < 4; ++e) fw[j][e] = tr_frag(lds, wa[e] + j * 32768, 2048);
    if (j < 2) {
      pq[2 * j] = *(const u32x4*)(lds + pa1 + j * 32768);
      pq[2 * j + 1] = *(const u32x4*)(lds + pa0 + j * 32768);
    }
  }
  TS(2);
  {
    int tries = 0;
    for (;;) {
      asm volatile("s_waitcnt vmcnt(0)" : "+v"(ka), "+v"(kc), "+v"(zc), "+v"(xr0), "+v"(xr1)::"memory");
      const unsigned m0 = max(max(ka[0], ka[1]), ka[2]), m1 = max(max(ka[3], kc[0]), kc[1]);
      const unsigned m2 = max(max(kc[2], kc[3]), zc[0]), m3 = max(max(zc[1], zc[2]), zc[3]);
      const unsigned mx = max(max(m0, m1), max(m2, m3));
      if (__builtin_amdgcn_ballot_w64(mx == 0xffffffffu) == 0) break;
      if (__builtin_expect(++tries > K2_TRIES, 0)) {
        const char* pk_ = ws + WS_KVP + sa * 4096 + l * 16;
        const char* pz_ = ws + WS_ZP + sa * 256 + ((l >> 2) & 1) * 128 + (l & 3) * 16;
        u32x2 r0 = red_slow(pk_, 32768), r1 = red_slow(pk_ + 1024, 32768);
        ka = (u32x4){r0[0], r0[1], r1[0], r1[1]};
        r0 = red_slow(pk_ + 2048, 32768); r1 = red_slow(pk_ + 3072, 32768);
        kc = (u32x4){r0[0], r0[1], r1[0], r1[1]};
        r0 = red_slow(pz_, 2048); r1 = red_slow(pz_ + 64, 2048);
        zc = (u32x4){r0[0], r0[1], r1[0], r1[1]};
        break;
      }
      K2_POLL();
    }
    *(u32x4*)(lds + K2_KV + w * 2048 + l * 16) = ka;
    *(u32x4*)(lds + K2_KV + w * 2048 + 1024 + l * 16) = kc;
    if (l < 8) *(u32x4*)(lds + K2_Z + w * 128 + l * 16) = zc;
  }
  TS(3);
  WAIT_L0();
  BAR();
  MEMF();
  TS(4);
  K2_RK(0);
  K2_RK(1);
  SB0();
  K2_ND(0);
  SB0();
  pq[0] = *(const u32x4*)(lds + pa1 + 65536);
  SB0();
  u32x4 of[2];
#pragma unroll
  for (int u = 0; u < 9; ++u) {
    if (u == 0 || u == 2) { SB0(); K2_ND(u + 1); SB0(); K2_RK(u / 2 + 2); }
    else if (u < 7) K2_ND(u + 1);
    if (u < 8) {
      const float r0 = __builtin_amdgcn_rcpf(dd[u & 1][0]), r1 = __builtin_amdgcn_rcpf(dd[u & 1][1]);
      of[u & 1][0] = pk2(n0[u & 1][0] * r0, n0[u & 1][1] * r0);
      of[u & 1][1] = pk2(n0[u & 1][2] * r0, n0[u & 1][3] * r0);
      of[u & 1][2] = pk2(n1[u & 1][0] * r1, n1[u & 1][1] * r1);
      of[u & 1][3] = pk2(n1[u & 1][2] * r1, n1[u & 1][3] * r1);
    }
    if (u > 0) {
#pragma unroll
      for (int e = 0; e < 4; ++e)
        acc[u & 1][3 - e] = mfma32(fw[(u - 1) >> 1][3 - e], as_bf8(of[(u - 1) & 1]), acc[u & 1][3 - e]);
    }
    if (u < 3) pq[u + 1] = *(const u32x4*)(lds + ((u & 1) ? pa1 : pa0) + ((u + 5) >> 1) * 32768);
#pragma unroll
    for (int t = 0; t < 7; ++t) {
      __builtin_amdgcn_sched_group_barrier(0x8, 1, 0);
      __builtin_amdgcn_sched_group_barrier(0x2, K2_VG, 0);
    }
    SB0();
    if (u == 7) {
      *(f32x4*)(lds + (w ^ 6) * 6144 + 4096 + l * 16) = acc[1][2];
      *(f32x4*)(lds + (w ^ 6) * 6144 + 5120 + l * 16) = acc[1][3];
      *(f32x4*)(lds + (w ^ 4) * 6144 + 2048 + l * 16) = acc[1][0];
      *(f32x4*)(lds + (w ^ 4) * 6144 + 3072 + l * 16) = acc[1][1];
    }
  }
  TS(5);
  *(f32x4*)(lds + (w ^ 2) * 6144 + l * 16) = acc[0][2];
  *(f32x4*)(lds + (w ^ 2) * 6144 + 1024 + l * 16) = acc[0][3];
  WAIT_L0();
  BAR();
  MEMF();
#pragma unroll
  for (int r = 0; r < 3; ++r) {
    acc[0][0] += *(const f32x4*)(lds + w * 6144 + r * 2048 + l * 16);
    acc[0][1] += *(const f32x4*)(lds + w * 6144 + r * 2048 + 1024 + l * 16);
  }
  u32x4 o;
  o[0] = pk2(bflo(xr0[0]) + bfr(acc[0][0][0]), bfhi(xr0[0]) + bfr(acc[0][0][1]));
  o[1] = pk2(bflo(xr0[1]) + bfr(acc[0][0][2]), bfhi(xr0[1]) + bfr(acc[0][0][3]));
  o[2] = pk2(bflo(xr1[0]) + bfr(acc[0][1][0]), bfhi(xr1[0]) + bfr(acc[0][1][1]));
  o[3] = pk2(bflo(xr1[1]) + bfr(acc[0][1][2]), bfhi(xr1[1]) + bfr(acc[0][1][3]));
  gst16(ws + WS_H + (size_t)((rt * 4 + mo) * 16 + 2 * q + kh) * 1024 + l * 16, o);
  {
    float s1 = 0.f, s2 = 0.f;
    stat_acc(o, s1, s2);
    auto p1 = __builtin_amdgcn_permlane32_swap(__builtin_bit_cast(unsigned, s1), __builtin_bit_cast(unsigned, s2), false, false);
    const unsigned p10 = p1[0], p11 = p1[1];
    float t = __builtin_bit_cast(float, p10) + __builtin_bit_cast(float, p11);
    auto p2 = __builtin_amdgcn_permlane16_swap(__builtin_bit_cast(unsigned, t), __builtin_bit_cast(unsigned, t), false, false);
    const unsigned p20 = p2[0], p21 = p2[1];
    t = __builtin_bit_cast(float, p20) + __builtin_bit_cast(float, p21);
    auto p3 = __builtin_amdgcn_permlane32_swap(__builtin_bit_cast(unsigned, t), __builtin_bit_cast(unsigned, t), false, false);
    const u32x2 sv = {p3[0], p3[1]};
    if (g == 0) gst8(ws + WS_ST + (size_t)((rt * 4 + mo) * 16 + 2 * q + kh) * 128 + i * 8, sv);
  }
  TS_END(2, 6, ws);
}

static constexpr int K3_ST = 4 * 32768;
static constexpr int K3_CS = K3_ST + 1024;
static constexpr int K3_LDS = K3_CS + 1024;

__global__ __launch_bounds__(768, 3) void k3_up(const char* __restrict__ w1, const char* __restrict__ b1,
                                                 const char* __restrict__ w2, char* __restrict__ ws) {
  __shared__ __attribute__((aligned(16))) char lds[K3_LDS];
  TS_DECL
  asm volatile("" ::"s"(w1), "s"(b1), "s"(w2), "s"(ws));
  const int tid = threadIdx.x, l = tid & 63, i = l & 15, g = l >> 4;
  const int w = __builtin_amdgcn_readfirstlane(tid >> 6);
  const int b = blockIdx.x, xcd = b & 7, idx = b >> 3;
  const int rhi = idx >= 14 ? 1 : 0;
  const int rt = xcd * 2 + rhi, cb = idx - 14 * rhi;
  const f32x4 z4 = {0.f, 0.f, 0.f, 0.f};

  if (idx >= 28) {
    if (w < 8) {
      const unsigned lds0 = (unsigned)(uintptr_t)(LDS_AS char*)lds;
      const char* p = w2 + ((idx - 28) * 8 + w) * 1024;
#pragma unroll 7
      for (int j = 0; j < WK3S; ++j) glds16(l * 16, p + j * 32768, lds0 + w * 1024);
      wait_vm<0>();
    }
    return;
  }

  if (w >= 8) {
    const int lw = w - 8;
    const int rot = (cb + 4 * rhi) & 7;
    const unsigned lds0 = (unsigned)(uintptr_t)(LDS_AS char*)lds;
    const unsigned v0 = l * 16, v1 = v0 + 1024, v2 = v0 + 16384, v3 = v0 + 17408;
    const unsigned u0 = g * 3584 + ((((l >> 1) & 7) ^ g) * 32) + (l & 1) * 16;
    const unsigned u1 = (u0 ^ 128) + 4 * 3584, u2 = u0 + 8 * 3584, u3 = u1 + 8 * 3584;
    const char* xb = ws + WS_H + (size_t)(rt * 8 + 2 * lw) * 16384;
    const char* wb = w1 + cb * 256 + lw * (16 * 3584);
    const unsigned ldx = lds0 + lw * 4096, ldw = lds0 + 16384 + lw * 4096;
#define K3_GX(S, V, JJ) glds16(V, xb + (((S) + rot) & 7) * 2048, ldx + ((S) & 3) * 32768 + (JJ) * 1024)
#define K3_GW(S, V, JJ) glds16(V, wb + (((S) + rot) & 7) * (64 * 3584), ldw + ((S) & 3) * 32768 + (JJ) * 1024)
#define K3_ST8(S) K3_GX(S, v0, 0); K3_GW(S, u0, 0); K3_GX(S, v2, 2); K3_GW(S, u1, 1); \
                  K3_GX(S, v1, 1); K3_GW(S, u2, 2); K3_GX(S, v3, 3); K3_GW(S, u3, 3)
    K3_ST8(0); K3_ST8(1);
    TS(1);
#define K3_SV 1
#define K3_SL 1
    u32x2 sp[8];
    float mean, var, mb, r1;
#pragma unroll
    for (int s = 0; s < 8; ++s) {
      if (s == K3_SL + 1 || s == K3_SL + 2) wait_vm<16>(); else if (s < 7) wait_vm<8>(); else wait_vm<WK3T / 2>();
      BAR();
      MEMF();
      if (s == 0) TS(2);
      if (s == 4) TS(3);
      SB0();
      if (s < 6) { K3_ST8(s + 2); }
      if (s >= 6) {
        const char* p = w2 + (1792 - 112 * WK3T + (idx * 4 + lw) * WK3T) * 1024;
#pragma unroll
        for (int j = (s == 6 ? 0 : WK3T / 2); j < (s == 6 ? WK3T / 2 : WK3T); ++j) glds16(v0, p + j * 1024, ldx);
      }
      SB0();
      if (s == K3_SL) {
        const char* sb = ws + WS_ST + (size_t)(rt * 8 + 2 * lw) * 2048;
#pragma unroll
        for (int j = 0; j < 8; ++j) gld8(sp[j], g * 1024 + i * 8, sb + j * 128);
      }
      if (s == K3_SL + 3) {
        asm volatile("" : "+v"(sp[0]), "+v"(sp[1]), "+v"(sp[2]), "+v"(sp[3]), "+v"(sp[4]), "+v"(sp[5]), "+v"(sp[6]), "+v"(sp[7]));
        f32x2 sv = __builtin_bit_cast(f32x2, sp[0]);
#pragma unroll
        for (int j = 1; j < 8; ++j) sv += __builtin_bit_cast(f32x2, sp[j]);
        const float sv0 = sv[0], sv1 = sv[1];
        auto p1 = __builtin_amdgcn_permlane16_swap(__builtin_bit_cast(unsigned, sv0), __builtin_bit_cast(unsigned, sv0), false, false);
        auto p2 = __builtin_amdgcn_permlane16_swap(__builtin_bit_cast(unsigned, sv1), __builtin_bit_cast(unsigned, sv1), false, false);
        const unsigned p10 = p1[0], p11 = p1[1], p20 = p2[0], p21 = p2[1];
        mean = (__builtin_bit_cast(float, p10) + __builtin_bit_cast(float, p11)) * (1.0f / 512.0f);
        var = fmaxf((__builtin_bit_cast(float, p20) + __builtin_bit_cast(float, p21)) * (1.0f / 512.0f) - mean * mean, 0.f);
        asm volatile("" : "+v"(mean), "+v"(var));
      }
      if (s == K3_SL + 4) {
        mb = bfr(mean);
        r1 = __builtin_amdgcn_rcpf(bfr(__builtin_amdgcn_sqrtf(bfr(bfr(var) + 1e-5f))));
        asm volatile("" : "+v"(mb), "+v"(r1));
      }
      if (s == K3_SL + 5) {
        const float mn2 = bfr((mean - mb) * r1);
        const float var2 = var * r1 * r1;
        const float r2 = __builtin_amdgcn_rcpf(bfr(__builtin_amdgcn_sqrtf(bfr(bfr(var2) + 1e-5f))));
        if ((g & 1) == 0) *(f32x2*)(lds + K3_ST + (32 * lw + 8 * g + i) * 8) = (f32x2){r1 * r2, (mb * r1 + mn2) * r2};
      }
      SB0();
    }
    TS(4);
    WAIT_L0();
    BAR();
    wait_vm<0>();
    TS_END(3, 5, ws);
    return;
  }

  const int rg = w & 3, nh = w >> 2, kf = rg >> 1, tx = 2 * (rg & 1);
  u32x2 bb[4];
#pragma unroll
  for (int e = 0; e < 4; ++e) gld8(bb[e], g * 8, b1 + cb * 256 + nh * 128 + (e ^ tx) * 32);
  int xa[2], wa[2][4];
  xa[0] = rg * 4096 + kf * 1024 + l * 16; xa[1] = xa[0] ^ 1024;
  {
    const int wr = 16384 + (kf * 32 + 4 * g + (i >> 2)) * 256 + (i & 3) * 8;
    const int f = 4 * (g & 1) + (i >> 2);
#pragma unroll
    for (int e = 0; e < 4; ++e) { wa[0][e] = wr + (((4 * nh + (e ^ tx)) ^ f) * 32); wa[1][e] = wa[0][e] ^ 8192; }
  }
  const u32x4 onesu = {0x3f803f80u, 0x3f803f80u, 0x3f803f80u, 0x3f803f80u};
  const bf16x8 ones = as_bf8(onesu);
  f32x4 acc[2][4], c0 = z4, c1 = z4;
#pragma unroll
  for (int m = 0; m < 2; ++m)
#pragma unroll
    for (int e = 0; e < 4; ++e) acc[m][e] = z4;
  bf16x8 fx[2][2], fw[2][4];
#define K3_HALF(C, RD, SLOT)                                                                                   \
  SB0();                                                                                                       \
  if (RD) fx[(C) ^ 1][0] = lds_b128(lds, xa[(C) ^ 1] + (SLOT) * 32768);                                        \
  SB0();                                                                                                       \
  acc[0][0] = mfma32(fw[C][0], fx[C][0], acc[0][0]);                                                           \
  SB0();                                                                                                       \
  if (RD) fw[(C) ^ 1][0] = tr_frag(lds, wa[(C) ^ 1][0] + (SLOT) * 32768, 4096);                                \
  SB0();                                                                                                       \
  acc[1][0] = mfma32(fw[C][0], fx[C][1], acc[1][0]);                                                           \
  SB0();                                                                                                       \
  if (RD) fx[(C) ^ 1][1] = lds_b128(lds, xa[(C) ^ 1] + (SLOT) * 32768 + 2048);                                 \
  SB0();                                                                                                       \
  acc[0][1] = mfma32(fw[C][1], fx[C][0], acc[0][1]);                                                           \
  SB0();                                                                                                       \
  if (RD) fw[(C) ^ 1][1] = tr_frag(lds, wa[(C) ^ 1][1] + (SLOT) * 32768, 4096);                                \
  SB0();                                                                                                       \
  acc[1][1] = mfma32(fw[C][1], fx[C][1], acc[1][1]);                                                           \
  SB0();                                                                                                       \
  if (RD) fw[(C) ^ 1][2] = tr_frag(lds, wa[(C) ^ 1][2] + (SLOT) * 32768, 4096);                                \
  SB0();                                                                                                       \
  acc[0][2] = mfma32(fw[C][2], fx[C][0], acc[0][2]);                                                           \
  SB0();                                                                                                       \
  if (RD) fw[(C) ^ 1][3] = tr_frag(lds, wa[(C) ^ 1][3] + (SLOT) * 32768, 4096);                                \
  SB0();                                                                                                       \
  acc[1][2] = mfma32(fw[C][2], fx[C][1], acc[1][2]);                                                           \
  acc[0][3] = mfma32(fw[C][3], fx[C][0], acc[0][3]);                                                           \
  acc[1][3] = mfma32(fw[C][3], fx[C][1], acc[1][3]);                                                           \
  if ((C) == 0) {                                                                                              \
    c0 = mfma32(fw[C][0], ones, c0);                                                                           \
    c1 = mfma32(fw[C][1], ones, c1);                                                                           \
  }                                                                                                            \
  SB0();
  TS(1);
  BAR();
  MEMF();
  TS(2);
  fx[0][0] = lds_b128(lds, xa[0]);
  fw[0][0] = tr_frag(lds, wa[0][0], 4096);
  fx[0][1] = lds_b128(lds, xa[0] + 2048);
  fw[0][1] = tr_frag(lds, wa[0][1], 4096);
  fw[0][2] = tr_frag(lds, wa[0][2], 4096);
  fw[0][3] = tr_frag(lds, wa[0][3], 4096);
#pragma unroll
  for (int s = 0; s < 8; ++s) {
    K3_HALF(0, true, s & 3);
    if (s == 7) {
      if (i == 0) {
        *(f32x4*)(lds + K3_CS + nh * 512 + kf * 256 + tx * 64 + g * 16) = c0;
        *(f32x4*)(lds + K3_CS + nh * 512 + kf * 256 + tx * 64 + 64 + g * 16) = c1;
      }
    }
    if (s < 7) { BAR(); MEMF(); }
    if (s == 3) TS(3);
    K3_HALF(1, s < 7, (s + 1) & 3);
  }
  TS(4);
  WAIT_L0();
  BAR();
  MEMF();
  asm volatile("s_waitcnt vmcnt(0)" : "+v"(bb[0]), "+v"(bb[1]), "+v"(bb[2]), "+v"(bb[3])::"memory");
  f32x2 gc1 = {-0.044715f * 2.302208198f, -0.044715f * 2.302208198f}, gc2 = {-2.302208198f, -2.302208198f}, gc3 = {1.0f, 1.0f};
  asm volatile("" : "+s"(gc1), "+v"(gc2), "+s"(gc3));
  f32x4 cs[4];
#pragma unroll
  for (int e = 0; e < 4; ++e) {
    const int ca = K3_CS + nh * 512 + (e ^ tx) * 64 + g * 16;
    cs[e] = *(const f32x4*)(lds + ca) + *(const f32x4*)(lds + ca + 256);
  }
#pragma unroll
  for (int m = 0; m < 2; ++m) {
    const f32x2 sc = *(const f32x2*)(lds + K3_ST + (32 * rg + 16 * m + i) * 8);
#pragma unroll
    for (int t = 0; t < 2; ++t) {
      u32x4 o;
#pragma unroll
      for (int e2 = 0; e2 < 2; ++e2) {
        const int e = 2 * t + e2;
#pragma unroll
        for (int r = 0; r < 2; ++r) {
          const unsigned bu = bb[e][r];
          const f32x2 bv = {bflo(bu), bfhi(bu)};
          const f32x2 av = {acc[m][e][2 * r], acc[m][e][2 * r + 1]}, cv = {cs[e][2 * r], cs[e][2 * r + 1]};
          const f32x2 tt = __builtin_elementwise_fma(av, (f32x2){sc[0], sc[0]}, __builtin_elementwise_fma((f32x2){-sc[1], -sc[1]}, cv, bv));
          const f32x2 u = tt * __builtin_elementwise_fma(tt * tt, gc1, gc2);
          const f32x2 ex = {__builtin_amdgcn_exp2f(u[0]), __builtin_amdgcn_exp2f(u[1])};
          const f32x2 dn = ex + gc3;
          const f32x2 gl = tt * (f32x2){__builtin_amdgcn_rcpf(dn[0]), __builtin_amdgcn_rcpf(dn[1])};
          o[2 * e2 + r] = pk2(gl[0], gl[1]);
        }
      }
      gst16(ws + WS_HM + (size_t)((rt * 8 + rg * 2 + m) * 56 + cb * 4 + nh * 2 + (t ^ (rg & 1))) * 1024 + l * 16, o);
    }
  }
  TS_END(3, 5, ws);
}

static constexpr int K4_LDS = 7 * 16384;

__global__ __launch_bounds__(512, 2) void k4_down(const char* __restrict__ w2, const char* __restrict__ b2,
                                                   const char* __restrict__ ws, char* __restrict__ out) {
  __shared__ __attribute__((aligned(16))) char lds[K4_LDS];
  TS_DECL
  asm volatile("" ::"s"(w2), "s"(b2), "s"(ws), "s"(out));
  const int tid = threadIdx.x, l = tid & 63, i = l & 15, g = l >> 4;
  const int w = __builtin_amdgcn_readfirstlane(tid >> 6);
  const int b = blockIdx.x, xcd = b & 7, idx = b >> 3;
  const int rt = xcd * 4 + (idx >> 3), q = idx & 7;
  const f32x4 z4 = {0.f, 0.f, 0.f, 0.f};

  if (w >= 4) {
    const int lw = w - 4;
    const int rot = idx >> 3;
    const unsigned lds0 = (unsigned)(uintptr_t)(LDS_AS char*)lds;
    const unsigned v0 = l * 16;
    const unsigned vw = (l >> 3) * 1024 + ((((l >> 1) & 3) ^ ((l >> 4) & 3)) * 32) + (l & 1) * 16;
    const char* xb = ws + WS_HM + (size_t)(rt * 4 + lw) * (56 * 1024);
    const char* wb = w2 + q * 128 + lw * 16384;
    const unsigned ld = lds0 + lw * 2048;
#define K4_ST(XB, WB, U)                                                                                       \
  do {                                                                                                         \
    glds16(v0, (XB) + (U) * 2048, ld + (U) * 16384);                                                           \
    glds16(vw, (WB) + (U) * 65536, ld + (U) * 16384 + 8192);                                                   \
    glds16(v0, (XB) + (U) * 2048 + 1024, ld + (U) * 16384 + 1024);                                             \
    glds16(vw, (WB) + (U) * 65536 + 8192, ld + (U) * 16384 + 8192 + 1024);                                     \
  } while (0)
    const char* xc = xb + rot * (7 * 2048);
    const char* wc = wb + rot * (7 * 65536);
#define X4_D 4
#pragma unroll
    for (int u = 0; u < X4_D; ++u) K4_ST(xc, wc, u);
    TS(1);
#pragma unroll 1
    for (int o = 0; o < 4; ++o) {
      const int nx = (o + 1 + rot) & 3;
      const char* xn = xb + nx * (7 * 2048);
      const char* wn = wb + nx * (7 * 65536);
#pragma unroll
      for (int u = 0; u < 7; ++u) {
        if (u < 8 - X4_D) wait_vm<4 * (X4_D - 1)>();
        else if (o < 3) wait_vm<4 * (X4_D - 1)>();
        else if (u == 6) wait_vm<0>();
        else if (u == 5) wait_vm<4>();
        else if (u == 4) wait_vm<8>();
        else wait_vm<12>();
        BAR();
        MEMF();
        if (u < 7 - X4_D) K4_ST(xc, wc, u + X4_D);
        else if (o < 3) K4_ST(xn, wn, u + X4_D - 7);
      }
      xc = xn; wc = wn;
    }
    TS(2);
    BAR();
    TS_END(4, 3, ws);
    return;
  }

  const int mh = w & 1, nh = w >> 1;
  u32x4 hf[2];
  u32x2 bb[2];
#pragma unroll
  for (int m = 0; m < 2; ++m) gld16(hf[m], l * 16, ws + WS_H + (size_t)((rt * 4 + 2 * mh + m) * 16 + 2 * q + nh) * 1024);
#pragma unroll
  for (int e = 0; e < 2; ++e) gld8(bb[e], g * 8, b2 + q * 128 + nh * 64 + e * 32);
  const int xa = mh * 4096 + l * 16;
  int wa[2];
  {
    const int wr = 8192 + (4 * g + (i >> 2)) * 128 + (i & 3) * 8;
    const int f = (2 * g + (i >> 3)) & 3;
    wa[0] = wr + (((2 * nh) ^ f) * 32); wa[1] = wa[0] ^ 32;
  }
  f32x4 acc[2][2] = {{z4, z4}, {z4, z4}};
  bf16x8 fx[2][2][2], fw[2][2][2];
#define K4_RD(B, U)                                                                                            \
  _Pragma("unroll") for (int ks = 0; ks < 2; ++ks) {                                                           \
    fx[B][ks][0] = lds_b128(lds, xa + (U) * 16384 + ks * 1024);                                                \
    fw[B][ks][0] = tr_frag(lds, wa[0] + (U) * 16384 + ks * 4096, 2048);                                        \
    fx[B][ks][1] = lds_b128(lds, xa + (U) * 16384 + ks * 1024 + 2048);                                         \
    fw[B][ks][1] = tr_frag(lds, wa[1] + (U) * 16384 + ks * 4096, 2048);                                        \
  }
#define K4_MM(B)                                                                                               \
  _Pragma("unroll") for (int ks = 0; ks < 2; ++ks) {                                                           \
    acc[0][0] = mfma32(fw[B][ks][0], fx[B][ks][0], acc[0][0]);                                                 \
    acc[1][0] = mfma32(fw[B][ks][0], fx[B][ks][1], acc[1][0]);                                                 \
    acc[0][1] = mfma32(fw[B][ks][1], fx[B][ks][0], acc[0][1]);                                                 \
    acc[1][1] = mfma32(fw[B][ks][1], fx[B][ks][1], acc[1][1]);                                                 \
  }
  TS(1);
  BAR();
  MEMF();
  K4_RD(0, 0);
#pragma unroll 1
  for (int o = 0; o < 2; ++o) {
#pragma unroll
    for (int u = 0; u < 14; ++u) {
      BAR();
      MEMF();
      SB0();
      K4_RD((u & 1) ^ 1, (u + 1) % 7);
      SB0();
      K4_MM(u & 1);
      SB0();
    }
  }
  TS(2);
  asm volatile("s_waitcnt vmcnt(0)" : "+v"(hf[0]), "+v"(hf[1]), "+v"(bb[0]), "+v"(bb[1])::"memory");
#pragma unroll
  for (int m = 0; m < 2; ++m) {
    unsigned pk[2][2];
#pragma unroll
    for (int e = 0; e < 2; ++e) {
      float ov[4];
#pragma unroll
      for (int r = 0; r < 4; ++r) {
        const unsigned hu = hf[m][2 * e + (r >> 1)], bu = bb[e][r >> 1];
        const float hval = (r & 1) ? bfhi(hu) : bflo(hu);
        const float bval = (r & 1) ? bfhi(bu) : bflo(bu);
        ov[r] = hval + bfr(bfr(acc[m][e][r]) + bval);
      }
      pk[e][0] = pk2(ov[0], ov[1]); pk[e][1] = pk2(ov[2], ov[3]);
    }
    auto s0 = __builtin_amdgcn_permlane16_swap(pk[0][0], pk[1][0], false, false);
    auto s1 = __builtin_amdgcn_permlane16_swap(pk[0][1], pk[1][1], false, false);
    const u32x4 o = {s0[0], s1[0], s0[1], s1[1]};
    const int row = rt * 64 + mh * 32 + m * 16 + i;
    gst16(out + (size_t)row * 1024 + q * 128 + nh * 64 + (g & 1) * 32 + (g >> 1) * 16, o);
  }
  TS_END(4, 3, ws);
}

__global__ __launch_bounds__(768, 3) void k1_qkv(const char* __restrict__ x, const char* __restrict__ wqkv,
                                                  const char* __restrict__ wo, const char* __restrict__ w1,
                                                  const char* __restrict__ w2, char* __restrict__ ws) {
  __shared__ __attribute__((aligned(16))) char lds[K1_LDS];
  TS_DECL
  asm volatile("" ::"s"(x), "s"(wqkv), "s"(wo), "s"(w1), "s"(w2), "s"(ws));
  const int tid = threadIdx.x, l = tid & 63, i = l & 15, g = l >> 4;
  const int w = __builtin_amdgcn_readfirstlane(tid >> 6);
  const int b = blockIdx.x, xcd = b & 7, idx = b >> 3;
  const int rt = (xcd >> 1) * 4 + (idx >> 3), hp = (xcd & 1) * 8 + (idx & 7);
  const f32x4 z4 = {0.f, 0.f, 0.f, 0.f};

  if (w >= 8) {
    const int lw = w - 8;
    const int rot = ((idx & 7) + 2 * (idx >> 3)) & 7;
    const unsigned lds0 = (unsigned)(uintptr_t)(LDS_AS char*)lds;
    unsigned vx[4], vw[3];
#pragma unroll
    for (int jj = 0; jj < 4; ++jj)
      vx[jj] = (32 * lw + 8 * jj + (l >> 3)) * 1024 + (((l & 7) ^ ((jj & 1) * 4 + (l >> 4))) * 16);
#pragma unroll
    for (int jj = 0; jj < 3; ++jj) {
      const int n = 64 * (3 * lw + jj) + l, row = n / 12, c = n - 12 * row;
      vw[jj] = row * 3072 + (c >> 2) * 1024 + ((((c >> 1) & 1) ^ ((row >> 3) & 1)) * 32) + (c & 1) * 16;
    }
    const char* xb = x + (size_t)rt * 131072;
    const char* wb = wqkv + hp * 64;
    const unsigned ldx = lds0 + lw * 4096, ldw = lds0 + K1_WOFF + lw * 3072;
#define K1_GX(S, JJ) glds16(vx[JJ], xb + (((S) + rot) & 7) * 128, ldx + ((S) & 3) * 16384 + (JJ) * 1024)
#define K1_GW(S, JJ) glds16(vw[JJ], wb + (size_t)(((S) + rot) & 7) * 196608, ldw + ((S) & 3) * 12288 + (JJ) * 1024)
#pragma unroll
    for (int s = 0; s < 2; ++s) {
      K1_GX(s, 0); K1_GW(s, 0); K1_GX(s, 1); K1_GW(s, 1); K1_GX(s, 2); K1_GW(s, 2); K1_GX(s, 3);
    }
    TS(1);
#pragma unroll
    for (int s = 0; s < 8; ++s) {
      if (s < 7) wait_vm<7>(); else wait_vm<W1K ? 4 : 0>();
      BAR();
      MEMF();
      if (s == 0) TS(2);
      if (s == 4) TS(3);
      SB0();
      if (s == 6) {
        const char* p = wo + (idx * 4 + lw) * 4096;
#pragma unroll
        for (int j = 0; j < 4; ++j) glds16(l * 16, p + j * 1024, ldx);
      }
      if (s == 7) {
        const int gw = min(b * 4 + lw, 895) * 2048;
        glds16(l * 16, w1 + gw, ldx); glds16(l * 16, w1 + gw + 1024, ldx);
        glds16(l * 16, w2 + gw, ldx); glds16(l * 16, w2 + gw + 1024, ldx);
      }
      if (s < 6) {
        K1_GX(s + 2, 0); K1_GW(s + 2, 0); K1_GX(s + 2, 1); K1_GW(s + 2, 1); K1_GX(s + 2, 2); K1_GW(s + 2, 2); K1_GX(s + 2, 3);
      }
      SB0();
    }
    TS(4);
    BAR();
    BAR();
    wait_vm<0>();
    TS_END(0, 5, ws);
    return;
  }

  const int rg = w & 3, h = w >> 2, kf = rg & 1;
  const u32x4 onesu = {0x3f803f80u, 0x3f803f80u, 0x3f803f80u, 0x3f803f80u};
  const bf16x8 ones = as_bf8(onesu);
  int xa[2][2], wa[2];
  {
    const int xr = (32 * rg + 16 * h + i) * 128 + ((g ^ ((i >> 1) & 7)) * 16);
    xa[0][0] = xr ^ (kf * 64); xa[0][1] = xa[0][0] ^ 2048;
    xa[1][0] = xa[0][0] ^ 64;  xa[1][1] = xa[1][0] ^ 2048;
    const int wr = K1_WOFF + (8 * g + (i >> 2)) * 192 + ((h ^ (g & 1)) * 32) + (i & 3) * 8;
    wa[0] = wr + kf * 6144; wa[1] = wr + (kf ^ 1) * 6144;
  }
  f32x4 aq[2] = {z4, z4}, ak[2] = {z4, z4}, av[2] = {z4, z4}, cq = z4, ck = z4, cv = z4, gs = z4, rs1 = z4;
  bf16x8 fx[2][2], fw[2][3];
#define K1_HALF(C, RD, SLOT, CS)                                                                               \
  SB0();                                                                                                       \
  if (RD) fx[(C) ^ 1][0] = lds_b128(lds, xa[(C) ^ 1][0] + (SLOT) * 16384);                                     \
  SB0();                                                                                                       \
  aq[0] = mfma32(fw[C][0], fx[C][0], aq[0]);                                                                   \
  SB0();                                                                                                       \
  if (RD) fw[(C) ^ 1][0] = tr_frag(lds, wa[(C) ^ 1] + (SLOT) * 12288, 768);                                    \
  SB0();                                                                                                       \
  aq[1] = mfma32(fw[C][0], fx[C][1], aq[1]);                                                                   \
  SB0();                                                                                                       \
  if (RD) fx[(C) ^ 1][1] = lds_b128(lds, xa[(C) ^ 1][1] + (SLOT) * 16384);                                     \
  SB0();                                                                                                       \
  ak[0] = mfma32(fx[C][0], fw[C][1], ak[0]);                                                                   \
  SB0();                                                                                                       \
  if (RD) fw[(C) ^ 1][1] = tr_frag(lds, wa[(C) ^ 1] + (SLOT) * 12288 + 64, 768);                               \
  SB0();                                                                                                       \
  ak[1] = mfma32(fx[C][1], fw[C][1], ak[1]);                                                                   \
  SB0();                                                                                                       \
  if (RD) fw[(C) ^ 1][2] = tr_frag(lds, wa[(C) ^ 1] + (SLOT) * 12288 + 128, 768);                              \
  SB0();                                                                                                       \
  av[0] = mfma32(fx[C][0], fw[C][2], av[0]);                                                                   \
  av[1] = mfma32(fx[C][1], fw[C][2], av[1]);                                                                   \
  gs = mfma32(fx[C][0], fx[C][0], gs);                                                                         \
  rs1 = mfma32(ones, fx[C][0], rs1);                                                                           \
  if (CS) {                                                                                                    \
    cq = mfma32(fw[C][0], ones, cq);                                                                           \
    ck = mfma32(ones, fw[C][1], ck);                                                                           \
    cv = mfma32(ones, fw[C][2], cv);                                                                           \
  }                                                                                                            \
  SB0();
  if (w == 7 && b < 17) gst16(ws + WS_KVF + b * 1024 + l * 16, (u32x4){~0u, ~0u, ~0u, ~0u});
  if (w == 6 && idx < CODE_KB) {
    const char* cp;
    int co;
    asm volatile("s_getpc_b64 %0\n\ts_mov_b32 %1, k2_attn@rel32@lo+4" : "=s"(cp), "=s"(co));
    cp += co;
    glds16(l * 16, cp + idx * 1024, (unsigned)(uintptr_t)(LDS_AS char*)lds + K1_KV);
  }
  TS(1);
  BAR();
  MEMF();
  TS(2);
  fx[0][0] = lds_b128(lds, xa[0][0]);
  fw[0][0] = tr_frag(lds, wa[0], 768);
  fx[0][1] = lds_b128(lds, xa[0][1]);
  fw[0][1] = tr_frag(lds, wa[0] + 64, 768);
  fw[0][2] = tr_frag(lds, wa[0] + 128, 768);
#pragma unroll
  for (int s = 0; s < 8; ++s) {
    K1_HALF(0, true, s & 3, true);
    if (s < 7) {
      BAR(); MEMF();
      if (s == 3) TS(3);
      K1_HALF(1, true, (s + 1) & 3, false);
    }
  }
  gs = mfma32(fx[1][0], fx[1][0], gs);
  rs1 = mfma32(ones, fx[1][0], rs1);
  SB0();
  aq[0] = mfma32(fw[1][0], fx[1][0], aq[0]);
  aq[1] = mfma32(fw[1][0], fx[1][1], aq[1]);
  ak[0] = mfma32(fx[1][0], fw[1][1], ak[0]);
  ak[1] = mfma32(fx[1][1], fw[1][1], ak[1]);
  av[0] = mfma32(fx[1][0], fw[1][2], av[0]);
  av[1] = mfma32(fx[1][1], fw[1][2], av[1]);
  {
    const float d01 = (i & 1) ? gs[1] : gs[0], d23 = (i & 1) ? gs[3] : gs[2];
    const float s2 = (i & 2) ? d23 : d01;
    const float mean = rs1[0] * (1.0f / 512.0f);
    const float var = fmaxf(s2 * (1.0f / 512.0f) - mean * mean, 0.f);
    const float rs = __builtin_amdgcn_rsqf(var + 1e-5f);
    if (g == (i >> 2)) *(f32x2*)(lds + K1_ST + (32 * rg + 16 * h + i) * 8) = (f32x2){rs, mean * rs};
  }
  TS(4);
  if (i == 0) *(f32x4*)(lds + K1_CS + w * 192 + g * 16) = cq;
  if (g == 0) {
    *(float*)(lds + K1_CS + w * 192 + 64 + i * 4) = ck[0];
    *(float*)(lds + K1_CS + w * 192 + 128 + i * 4) = cv[0];
  }
  wait_vm<0>();
  WAIT_L0();
  BAR();
  MEMF();
  cq += *(const f32x4*)(lds + K1_CS + (w ^ 1) * 192 + g * 16);
  const float csk = ck[0] + *(const float*)(lds + K1_CS + (w ^ 1) * 192 + 64 + i * 4);
  const float csv = cv[0] + *(const float*)(lds + K1_CS + (w ^ 1) * 192 + 128 + i * 4);
  u32x4 pq, pk, pv;
#pragma unroll
  for (int m = 0; m < 2; ++m) {
    const int rb = K1_ST + (32 * rg + 16 * (h ^ m)) * 8;
    const f32x2 sq = *(const f32x2*)(lds + rb + i * 8);
    const f32x4 s01 = *(const f32x4*)(lds + rb + g * 32);
    const f32x4 s23 = *(const f32x4*)(lds + rb + g * 32 + 16);
    float q[4], k[4], v[4];
#pragma unroll
    for (int r = 0; r < 4; ++r) {
      const float rs = r == 0 ? s01[0] : (r == 1 ? s01[2] : (r == 2 ? s23[0] : s23[2]));
      const float mr = r == 0 ? s01[1] : (r == 1 ? s01[3] : (r == 2 ? s23[1] : s23[3]));
      q[r] = phi_p(__builtin_fmaf(aq[m][r], sq[0], __builtin_fmaf(-sq[1], cq[r], 1e-6f)));
      k[r] = phi_p(__builtin_fmaf(ak[m][r], rs, __builtin_fmaf(-mr, csk, 1e-6f)));
      v[r] = __builtin_fmaf(av[m][r], rs, -mr * csv);
    }
    pq[2 * m] = pk2(q[0], q[1]); pq[2 * m + 1] = pk2(q[2], q[3]);
    pk[2 * m] = pk2(k[0], k[1]); pk[2 * m + 1] = pk2(k[2], k[3]);
    pv[2 * m] = pk2(v[0], v[1]); pv[2 * m + 1] = pk2(v[2], v[3]);
  }
  const f32x4 kvt = mfma32(as_bf8(pk), as_bf8(pv), z4);
  const f32x4 zz = mfma32(as_bf8(pk), ones, z4);
  {
    char* pd = ws + WS_PQ + (size_t)(((rt * 4 + rg) * 2 + h) * 16 + hp) * 1024 + l * 16 + h * 8;
    gst8(pd, (u32x2){pq[0], pq[1]});
    gst8(pd + (1 - 2 * h) * 16384, (u32x2){pq[2], pq[3]});
  }
  *(f32x4*)(lds + K1_KV + w * 1024 + l * 16) = kvt;
  if (i == 0) *(f32x4*)(lds + K1_Z + w * 64 + g * 16) = zz;
  WAIT_L0();
  BAR();
  MEMF();
  if (w < 2) {
    f32x4 s = *(const f32x4*)(lds + K1_KV + w * 4096 + l * 16);
#pragma unroll
    for (int r = 1; r < 4; ++r) s += *(const f32x4*)(lds + K1_KV + w * 4096 + r * 1024 + l * 16);
    gst16(ws + WS_KVP + (size_t)(rt * 32 + 2 * hp + w) * 1024 + l * 16, __builtin_bit_cast(u32x4, s));
  } else if (w == 2 && l < 8) {
    f32x4 s = *(const f32x4*)(lds + K1_Z + (l >> 2) * 256 + (l & 3) * 16);
#pragma unroll
    for (int r = 1; r < 4; ++r) s += *(const f32x4*)(lds + K1_Z + (l >> 2) * 256 + r * 64 + (l & 3) * 16);
    gst16(ws + WS_ZP + (size_t)(rt * 32 + 2 * hp) * 64 + l * 16, __builtin_bit_cast(u32x4, s));
  }
  TS_END(0, 5, ws);
}

extern "C" void kernel_launch(void* const* d_in, const int* in_sizes, int n_in, void* d_out, int out_size,
                              void* d_ws, size_t ws_size, hipStream_t stream) {
  const char* x = (const char*)d_in[0];
  const char* wqkv = (const char*)d_in[1];
  const char* wo = (const char*)d_in[2];
  const char* w1 = (const char*)d_in[4];
  const char* b1 = (const char*)d_in[5];
  const char* w2 = (const char*)d_in[6];
  const char* b2 = (const char*)d_in[7];
  char* ws = (char*)d_ws;
  k1_qkv<<<256, 768, 0, stream>>>(x, wqkv, wo, w1, w2, ws);
  k2_attn<<<256, 768, 0, stream>>>(x, wo, w1, ws);
  k3_up<<<WK3S ? 256 : 224, 768, 0, stream>>>(w1, b1, w2, ws);
  k4_down<<<256, 512, 0, stream>>>(w2, b2, ws, (char*)d_out);
}
```

```cpp
#include <hip/hip_runtime.h>
#include <hip/hip_bf16.h>
#include <stdint.h>

typedef __attribute__((ext_vector_type(8))) short bf16x8;
typedef __attribute__((ext_vector_type(4))) short bf16x4;
typedef __attribute__((ext_vector_type(4))) short v4i16_t;
typedef __attribute__((ext_vector_type(4))) float f32x4;
typedef __attribute__((ext_vector_type(2))) float f32x2;
typedef __attribute__((ext_vector_type(4))) unsigned u32x4;
typedef __attribute__((ext_vector_type(2))) unsigned u32x2;
typedef __attribute__((ext_vector_type(2))) __bf16 bfv2;

#define DEV __device__ __forceinline__
#define LDS_AS __attribute__((address_space(3)))

static constexpr size_t WS_PQ  = 0;
static constexpr size_t WS_H   = WS_PQ + (2u << 20);
static constexpr size_t WS_HM  = WS_H + (2u << 20);
static constexpr size_t WS_KVP = WS_HM + (7u << 20);
static constexpr size_t WS_ZP  = WS_KVP + (512u << 10);
static constexpr size_t WS_KVF = WS_ZP + (32u << 10);
static constexpr size_t WS_ZF  = WS_KVF + (16u << 10);
static constexpr size_t WS_ST  = WS_KVF + (32u << 10);

#define W1K 1
#define WKR 3
#define WK2 11
#define K2_DLY 0
#define CPF 1
#define CODE_KB 24
#define K2_TRIES 2000
#define K2_B0 6
#define K2_B1 7
#define K2_PJ 2
#define K2_VG 2
#define WK3S 42
#define WK3T 4

DEV f32x4 mfma32(bf16x8 a, bf16x8 b, f32x4 c) { return __builtin_amdgcn_mfma_f32_16x16x32_bf16(a, b, c, 0, 0, 0); }
DEV f32x4 mfma16(bf16x4 a, bf16x4 b, f32x4 c) { return __builtin_amdgcn_mfma_f32_16x16x16bf16_1k(a, b, c, 0, 0, 0); }

DEV unsigned pk2(float a, float b) {
  f32x2 v = {a, b};
  bfv2 r = __builtin_convertvector(v, bfv2);
  return __builtin_bit_cast(unsigned, r);
}
DEV float bflo(unsigned u) { return __builtin_bit_cast(float, u << 16); }
DEV float bfhi(unsigned u) { return __builtin_bit_cast(float, u & 0xffff0000u); }
DEV float bfr(float a) { return bflo(pk2(a, a)); }

DEV u32x4 ldg16(const void* p) { return *(const u32x4*)p; }
DEV u32x2 ldg8(const void* p) { return *(const u32x2*)p; }
DEV bf16x8 as_bf8(u32x4 v) { return __builtin_bit_cast(bf16x8, v); }
DEV bf16x4 as_bf4(u32x2 v) { return __builtin_bit_cast(bf16x4, v); }

DEV float phi_f(float t) {
  float e = __builtin_amdgcn_exp2f(t * 1.44269504088896341f);
  return (t > 0.f ? t + 1.f : e) + 1e-6f;
}

#define WAIT_V(n) asm volatile("s_waitcnt vmcnt(" #n ")" ::: "memory")
#define WAIT_L0() asm volatile("s_waitcnt lgkmcnt(0)" ::: "memory")
#define BAR() __builtin_amdgcn_s_barrier()

#define TS_DECL
#define TS(k)
#define TS_END(kid, n, wsp)
template <int N> DEV void wait_vm() { asm volatile("s_waitcnt vmcnt(%0)" ::"n"(N) : "memory"); }

DEV void ln_scalars(float mean, float var, float& a, float& b) {
  float mb = bfr(mean);
  float d = bfr(__builtin_sqrtf(bfr(bfr(var) + 1e-5f)));
  a = 1.0f / d;
  b = mb * a;
}

static constexpr int K1_WOFF = 4 * 16384;
static constexpr int K1_ST = K1_WOFF + 4 * 12288;
static constexpr int K1_CS = K1_ST + 1024;
static constexpr int K1_KV = K1_CS + 2048;
static constexpr int K1_Z = K1_KV + 8192;
static constexpr int K1_LDS = K1_Z + 512;

DEV void glds16(unsigned voff, const char* sbase, unsigned ldsdst) {
  unsigned keep;
  asm volatile("s_mov_b32 %0, m0\n\ts_mov_b32 m0, %3\n\ts_nop 0\n\tglobal_load_lds_dwordx4 %1, %2\n\ts_mov_b32 m0, %0"
               : "=&s"(keep) : "v"(voff), "s"(sbase), "s"(ldsdst) : "memory");
}
DEV void gld16(u32x4& d, unsigned voff, const char* sbase) {
  asm volatile("global_load_dwordx4 %0, %1, %2" : "=v"(d) : "v"(voff), "s"(sbase) : "memory");
}
DEV void gld8(u32x2& d, unsigned voff, const char* sbase) {
  asm volatile("global_load_dwordx2 %0, %1, %2" : "=v"(d) : "v"(voff), "s"(sbase) : "memory");
}
#define ST_POL 3
#define ST_SFX " sc0 sc1"
DEV void gst16(char* p, u32x4 v) {
  asm volatile("global_store_dwordx4 %0, %1, off" ST_SFX "\n\ts_nop 1" ::"v"(p), "v"(v) : "memory");
}
DEV void gst8(char* p, u32x2 v) {
  asm volatile("global_store_dwordx2 %0, %1, off" ST_SFX ::"v"(p), "v"(v) : "memory");
}
#define SB0() __builtin_amdgcn_sched_barrier(0)
#define MEMF() asm volatile("" ::: "memory")
DEV unsigned perm_lo(unsigned a, unsigned b) { return __builtin_amdgcn_perm(b, a, 0x05040100u); }
DEV unsigned perm_hi(unsigned a, unsigned b) { return __builtin_amdgcn_perm(b, a, 0x07060302u); }
DEV bf16x8 lds_b128(const char* lds, int off) { return *(const bf16x8*)(lds + off); }
DEV bf16x8 tr_frag(const char* lds, int off, int hi) {
  v4i16_t a = __builtin_amdgcn_ds_read_tr16_b64_v4i16((LDS_AS v4i16_t*)(lds + off));
  v4i16_t b = __builtin_amdgcn_ds_read_tr16_b64_v4i16((LDS_AS v4i16_t*)(lds + off + hi));
  return __builtin_shufflevector(a, b, 0, 1, 2, 3, 4, 5, 6, 7);
}
DEV float phi_p(float tp) {
  const float u = __builtin_fmaf(tp, 1.44269504088896341f, -1.44269504088896341e-6f);
  const float e = __builtin_amdgcn_fmed3f(__builtin_amdgcn_exp2f(u), 0.f, 1.f);
  return fmaxf(tp, 1e-6f) + e;
}
DEV void stat_acc(u32x4 v, float& s1, float& s2) {
  const bfv2 one = __builtin_bit_cast(bfv2, 0x3f803f80u);
#pragma unroll
  for (int d = 0; d < 4; ++d) {
    const unsigned au = v[d];
    const bfv2 a = __builtin_bit_cast(bfv2, au);
    s1 = __builtin_amdgcn_fdot2_f32_bf16(a, one, s1, false);
    s2 = __builtin_amdgcn_fdot2_f32_bf16(a, a, s2, false);
  }
}

static constexpr int K2_KV = 8 * 16384;
static constexpr int K2_Z = K2_KV + 16384;
static constexpr int K2_SCR = K2_Z + 1024;
static constexpr int K2_LDS = K2_SCR + 4096;

DEV unsigned not_ones(unsigned v) { return v == 0xffffffffu ? 0x7fc07fc0u : v; }
DEV u32x2 red_slow(const char* p, int stride) {
  f32x4 s = {0.f, 0.f, 0.f, 0.f};
#pragma unroll 1
  for (int r = 0; r < 16; ++r) s += *(const f32x4*)(p + r * stride);
  return (u32x2){pk2(s[0], s[1]), pk2(s[2], s[3])};
}

extern "C" __global__ __launch_bounds__(768, 3) void k2_attn(const char* __restrict__ x, const char* __restrict__ wo,
                                                   const char* __restrict__ w1, char* __restrict__ ws) {
  __shared__ __attribute__((aligned(16))) char lds[K2_LDS];
  TS_DECL
  asm volatile("" ::"s"(x), "s"(wo), "s"(w1), "s"(ws));
  const int tid = threadIdx.x, l = tid & 63, i = l & 15, g = l >> 4;
  const int w = __builtin_amdgcn_readfirstlane(tid >> 6);
  const int b = blockIdx.x, xcd = b & 7, idx = b >> 3;
  const int rt = xcd * 4 + (idx >> 3), q = idx & 7;
  const int rot = 2 * (((idx >> 3) + q) & 3);
  const f32x4 z4 = {0.f, 0.f, 0.f, 0.f};

  if (w >= 8) {
    const int lw = w - 8;
    const unsigned lds0 = (unsigned)(uintptr_t)(LDS_AS char*)lds;
    const unsigned v0 = l * 16;
    const unsigned vw = (l >> 3) * 1024 + ((((l >> 1) & 3) ^ ((l >> 4) & 3)) * 32) + (l & 1) * 16;
    const char* pb = ws + WS_PQ + (size_t)(rt * 4 + lw) * 16384;
    const char* wb = wo + q * 128 + lw * 16384;
    const unsigned ld = lds0 + lw * 2048;
#pragma unroll
    for (int s = 0; s < 8; ++s) {
      const int sa_ = (s + rot) & 7;
      if (s == K2_B0) { wait_vm<4 * K2_B0 - 8>(); BAR(); }
      if (s == K2_B1) { wait_vm<4 * K2_B1 - 16>(); BAR(); }
      glds16(v0, pb + sa_ * 2048, ld + s * 16384);
      glds16(vw, wb + sa_ * 65536, ld + s * 16384 + 8192);
      glds16(v0, pb + sa_ * 2048 + 1024, ld + s * 16384 + 1024);
      glds16(vw, wb + sa_ * 65536 + 8192, ld + s * 16384 + 8192 + 1024);
    }
    const char* pw = w1 + (idx * 4 + lw) * 14336;
#pragma unroll
    for (int j = 0; j < WKR; ++j) glds16(v0, pw + j * 1024, lds0 + K2_SCR + lw * 1024);
    TS(1);
    TS(2);
    if (K2_B0 > 7) { wait_vm<24 + WKR>(); BAR(); }
    if (K2_B1 > 7) { wait_vm<16 + WKR>(); BAR(); }
    wait_vm<8 + WKR>(); BAR();
    wait_vm<WKR>();
    TS(3);
    BAR();
    BAR();
    MEMF();
    TS(4);
#pragma unroll
    for (int j = 14 - WK2; j < 14; ++j) glds16(v0, pw + j * 1024, lds0 + K2_SCR + lw * 1024);
    BAR();
    wait_vm<0>();
    TS_END(2, 5, ws);
    return;
  }

  const int mp = w & 1, kq = w >> 1, kh = kq & 1, mo = 2 * mp + (kq >> 1);
  if (b < 160 && w == 0) {
    const bool isk = b < 128;
    const int hd = isk ? b >> 2 : b - 128;
    const char* pp = isk ? ws + WS_KVP + hd * 1024 + (b & 3) * 256 : ws + WS_ZP + hd * 64;
    const unsigned pv = isk ? g * 32768 + i * 16 : g * 2048 + (i & 3) * 16;
    const int ps = isk ? 131072 : 8192;
    u32x4 t[4];
#pragma unroll
    for (int r = 0; r < 4; ++r) gld16(t[r], pv, pp + r * ps);
    asm volatile("s_waitcnt vmcnt(0)" : "+v"(t[0]), "+v"(t[1]), "+v"(t[2]), "+v"(t[3])::"memory");
    f32x4 sm = (__builtin_bit_cast(f32x4, t[0]) + __builtin_bit_cast(f32x4, t[1])) +
               (__builtin_bit_cast(f32x4, t[2]) + __builtin_bit_cast(f32x4, t[3]));
    float sv[4];
#pragma unroll
    for (int r = 0; r < 4; ++r) {
      const float f = sm[r];
      auto p1 = __builtin_amdgcn_permlane16_swap(__builtin_bit_cast(unsigned, f), __builtin_bit_cast(unsigned, f), false, false);
      const unsigned p10 = p1[0], p11 = p1[1];
      const float f2 = __builtin_bit_cast(float, p10) + __builtin_bit_cast(float, p11);
      auto p2 = __builtin_amdgcn_permlane32_swap(__builtin_bit_cast(unsigned, f2), __builtin_bit_cast(unsigned, f2), false, false);
      const unsigned p20 = p2[0], p21 = p2[1];
      sv[r] = __builtin_bit_cast(float, p20) + __builtin_bit_cast(float, p21);
    }
    const u32x2 pv2 = {not_ones(pk2(sv[0], sv[1])), not_ones(pk2(sv[2], sv[3]))};
    char* pd = ws + (isk ? WS_KVF + (hd >> 1) * 1024 + (b & 3) * 256 : WS_ZF + (hd >> 1) * 64) + (hd & 1) * 8 + l * 16;
    if (l < (isk ? 16 : 4)) asm volatile("global_store_dwordx2 %0, %1, off sc1" ::"v"(pd), "v"(pv2) : "memory");
  }
  u32x2 xr0, xr1;
  {
    const char* xp = x + (size_t)(rt * 64 + mo * 16) * 1024 + q * 128 + kh * 64;
    gld8(xr0, i * 1024 + g * 8, xp);
    gld8(xr1, i * 1024 + g * 8 + 32, xp);
  }
  const int sb = (kq >> 1) * 16384;
  const int pa0 = sb + (mo * 2 + kh) * 1024 + l * 16, pa1 = pa0 ^ 2048;
  const int kva = K2_KV + kq * 1024 + l * 16, za = K2_Z + kq * 64 + g * 16;
  int wa[4];
  {
    const int wr = sb + 8192 + (kh * 32 + 4 * g + (i >> 2)) * 128 + (i & 3) * 8;
    const int f = (2 * g + (i >> 3)) & 3;
#pragma unroll
    for (int e = 0; e < 4; ++e) wa[e] = wr + ((e ^ (2 * kh) ^ f) * 32);
  }
  const f32x4 eps4 = {1e-6f, 1e-6f, 1e-6f, 1e-6f};
  f32x4 acc[2][4] = {{z4, z4, z4, z4}, {z4, z4, z4, z4}};
  bf16x8 fw[4][4];
  u32x4 pq[4];
  u32x4 kv[2], zf[2];
  f32x4 dd[2], n0[2], n1[2];
  const unsigned mza = (i & 3) == 0 ? ~0u : 0u, mzb = (i & 3) == 1 ? ~0u : 0u;
#define K2_RK(J) do { kv[(J) & 1] = *(const u32x4*)(lds + kva + (J) * 4096); zf[(J) & 1] = *(const u32x4*)(lds + za + (J) * 256); } while (0)
#define K2_ND(U)                                                                                               \
  do {                                                                                                         \
    const u32x4 zq_ = zf[((U) >> 1) & 1], kq_ = kv[((U) >> 1) & 1];                                            \
    dd[(U) & 1] = mfma32(as_bf8((u32x4){zq_[0] & mza, zq_[1] & mza, zq_[2] & mzb, zq_[3] & mzb}), as_bf8(pq[(U) & 3]), eps4); \
    n0[(U) & 1] = mfma32(as_bf8((u32x4){kq_[0], kq_[1], 0u, 0u}), as_bf8(pq[(U) & 3]), z4);                     \
    n1[(U) & 1] = mfma32(as_bf8((u32x4){0u, 0u, kq_[2], kq_[3]}), as_bf8(pq[(U) & 3]), z4);                     \
  } while (0)
  const int sa = (w + rot) & 7;
  const char* kp = ws + WS_KVF + sa * 2048;
  const char* zp = ws + WS_ZF + sa * 128;
  u32x4 ka, kc, zc;
#define K2_POLL()                                                                                              \
  asm volatile("global_load_dwordx4 %0, %3, %4 sc1\n\tglobal_load_dwordx4 %1, %3, %4 offset:1024 sc1\n\t"       \
               "global_load_dwordx4 %2, %5, %6 sc1"                                                            \
               : "=&v"(ka), "=&v"(kc), "=&v"(zc) : "v"(l * 16), "s"(kp), "v"((l & 7) * 16), "s"(zp) : "memory")
  TS(1);
#pragma unroll
  for (int j = 0; j < 4; ++j) {
    BAR();
    MEMF();
    if (j == K2_PJ) K2_POLL();
#pragma unroll
    for (int e = 0; e < 4; ++e) fw[j][e] = tr_frag(lds, wa[e] + j * 32768, 2048);
    if (j < 2) {
      pq[2 * j] = *(const u32x4*)(lds + pa1 + j * 32768);
      pq[2 * j + 1] = *(const u32x4*)(lds + pa0 + j * 32768);
    }
  }
  TS(2);
  {
    int tries = 0;
    for (;;) {
      asm volatile("s_waitcnt vmcnt(0)" : "+v"(ka), "+v"(kc), "+v"(zc), "+v"(xr0), "+v"(xr1)::"memory");
      const unsigned m0 = max(max(ka[0], ka[1]), ka[2]), m1 = max(max(ka[3], kc[0]), kc[1]);
      const unsigned m2 = max(max(kc[2], kc[3]), zc[0]), m3 = max(max(zc[1], zc[2]), zc[3]);
      const unsigned mx = max(max(m0, m1), max(m2, m3));
      if (__builtin_amdgcn_ballot_w64(mx == 0xffffffffu) == 0) break;
      if (__builtin_expect(++tries > K2_TRIES, 0)) {
        const char* pk_ = ws + WS_KVP + sa * 4096 + l * 16;
        const char* pz_ = ws + WS_ZP + sa * 256 + ((l >> 2) & 1) * 128 + (l & 3) * 16;
        u32x2 r0 = red_slow(pk_, 32768), r1 = red_slow(pk_ + 1024, 32768);
        ka = (u32x4){r0[0], r0[1], r1[0], r1[1]};
        r0 = red_slow(pk_ + 2048, 32768); r1 = red_slow(pk_ + 3072, 32768);
        kc = (u32x4){r0[0], r0[1], r1[0], r1[1]};
        r0 = red_slow(pz_, 2048); r1 = red_slow(pz_ + 64, 2048);
        zc = (u32x4){r0[0], r0[1], r1[0], r1[1]};
        break;
      }
      K2_POLL();
    }
    *(u32x4*)(lds + K2_KV + w * 2048 + l * 16) = ka;
    *(u32x4*)(lds + K2_KV + w * 2048 + 1024 + l * 16) = kc;
    if (l < 8) *(u32x4*)(lds + K2_Z + w * 128 + l * 16) = zc;
  }
  TS(3);
  WAIT_L0();
  BAR();
  MEMF();
  TS(4);
  K2_RK(0);
  K2_RK(1);
  SB0();
  K2_ND(0);
  SB0();
  pq[0] = *(const u32x4*)(lds + pa1 + 65536);
  SB0();
  u32x4 of[2];
#pragma unroll
  for (int u = 0; u < 9; ++u) {
    if (u == 0 || u == 2) { SB0(); K2_ND(u + 1); SB0(); K2_RK(u / 2 + 2); }
    else if (u < 7) K2_ND(u + 1);
    if (u < 8) {
      const float r0 = __builtin_amdgcn_rcpf(dd[u & 1][0]), r1 = __builtin_amdgcn_rcpf(dd[u & 1][1]);
      of[u & 1][0] = pk2(n0[u & 1][0] * r0, n0[u & 1][1] * r0);
      of[u & 1][1] = pk2(n0[u & 1][2] * r0, n0[u & 1][3] * r0);
      of[u & 1][2] = pk2(n1[u & 1][0] * r1, n1[u & 1][1] * r1);
      of[u & 1][3] = pk2(n1[u & 1][2] * r1, n1[u & 1][3] * r1);
    }
    if (u > 0) {
#pragma unroll
      for (int e = 0; e < 4; ++e)
        acc[u & 1][3 - e] = mfma32(fw[(u - 1) >> 1][3 - e], as_bf8(of[(u - 1) & 1]), acc[u & 1][3 - e]);
    }
    if (u < 3) pq[u + 1] = *(const u32x4*)(lds + ((u & 1) ? pa1 : pa0) + ((u + 5) >> 1) * 32768);
#pragma unroll
    for (int t = 0; t < 7; ++t) {
      __builtin_amdgcn_sched_group_barrier(0x8, 1, 0);
      __builtin_amdgcn_sched_group_barrier(0x2, K2_VG, 0);
    }
    SB0();
    if (u == 7) {
      *(f32x4*)(lds + (w ^ 6) * 6144 + 4096 + l * 16) = acc[1][2];
      *(f32x4*)(lds + (w ^ 6) * 6144 + 5120 + l * 16) = acc[1][3];
      *(f32x4*)(lds + (w ^ 4) * 6144 + 2048 + l * 16) = acc[1][0];
      *(f32x4*)(lds + (w ^ 4) * 6144 + 3072 + l * 16) = acc[1][1];
    }
  }
  TS(5);
  *(f32x4*)(lds + (w ^ 2) * 6144 + l * 16) = acc[0][2];
  *(f32x4*)(lds + (w ^ 2) * 6144 + 1024 + l * 16) = acc[0][3];
  WAIT_L0();
  BAR();
  MEMF();
#pragma unroll
  for (int r = 0; r < 3; ++r) {
    acc[0][0] += *(const f32x4*)(lds + w * 6144 + r * 2048 + l * 16);
    acc[0][1] += *(const f32x4*)(lds + w * 6144 + r * 2048 + 1024 + l * 16);
  }
  u32x4 o;
  o[0] = pk2(bflo(xr0[0]) + bfr(acc[0][0][0]), bfhi(xr0[0]) + bfr(acc[0][0][1]));
  o[1] = pk2(bflo(xr0[1]) + bfr(acc[0][0][2]), bfhi(xr0[1]) + bfr(acc[0][0][3]));
  o[2] = pk2(bflo(xr1[0]) + bfr(acc[0][1][0]), bfhi(xr1[0]) + bfr(acc[0][1][1]));
  o[3] = pk2(bflo(xr1[1]) + bfr(acc[0][1][2]), bfhi(xr1[1]) + bfr(acc[0][1][3]));
  gst16(ws + WS_H + (size_t)((rt * 4 + mo) * 16 + 2 * q + kh) * 1024 + l * 16, o);
  {
    float s1 = 0.f, s2 = 0.f;
    stat_acc(o, s1, s2);
    auto p1 = __builtin_amdgcn_permlane32_swap(__builtin_bit_cast(unsigned, s1), __builtin_bit_cast(unsigned, s2), false, false);
    const unsigned p10 = p1[0], p11 = p1[1];
    float t = __builtin_bit_cast(float, p10) + __builtin_bit_cast(float, p11);
    auto p2 = __builtin_amdgcn_permlane16_swap(__builtin_bit_cast(unsigned, t), __builtin_bit_cast(unsigned, t), false, false);
    const unsigned p20 = p2[0], p21 = p2[1];
    t = __builtin_bit_cast(float, p20) + __builtin_bit_cast(float, p21);
    auto p3 = __builtin_amdgcn_permlane32_swap(__builtin_bit_cast(unsigned, t), __builtin_bit_cast(unsigned, t), false, false);
    const u32x2 sv = {p3[0], p3[1]};
    if (g == 0) gst8(ws + WS_ST + (size_t)((rt * 4 + mo) * 16 + 2 * q + kh) * 128 + i * 8, sv);
  }
  TS_END(2, 6, ws);
}

static constexpr int K3_ST = 4 * 32768;
static constexpr int K3_CS = K3_ST + 1024;
static constexpr int K3_LDS = K3_CS + 1024;

__global__ __launch_bounds__(768, 3) void k3_up(const char* __restrict__ w1, const char* __restrict__ b1,
                                                 const char* __restrict__ w2, char* __restrict__ ws) {
  __shared__ __attribute__((aligned(16))) char lds[K3_LDS];
  TS_DECL
  asm volatile("" ::"s"(w1), "s"(b1), "s"(w2), "s"(ws));
  const int tid = threadIdx.x, l = tid & 63, i = l & 15, g = l >> 4;
  const int w = __builtin_amdgcn_readfirstlane(tid >> 6);
  const int b = blockIdx.x, xcd = b & 7, idx = b >> 3;
  const int rhi = idx >= 14 ? 1 : 0;
  const int rt = xcd * 2 + rhi, cb = idx - 14 * rhi;
  const f32x4 z4 = {0.f, 0.f, 0.f, 0.f};

  if (idx >= 28) {
    if (w < 8) {
      const unsigned lds0 = (unsigned)(uintptr_t)(LDS_AS char*)lds;
      const char* p = w2 + ((idx - 28) * 8 + w) * 1024;
#pragma unroll 7
      for (int j = 0; j < WK3S; ++j) glds16(l * 16, p + j * 32768, lds0 + w * 1024);
      wait_vm<0>();
    }
    return;
  }

  if (w >= 8) {
    const int lw = w - 8;
    const int rot = (cb + 4 * rhi) & 7;
    const unsigned lds0 = (unsigned)(uintptr_t)(LDS_AS char*)lds;
    const unsigned v0 = l * 16, v1 = v0 + 1024, v2 = v0 + 16384, v3 = v0 + 17408;
    const unsigned u0 = g * 3584 + ((((l >> 1) & 7) ^ g) * 32) + (l & 1) * 16;
    const unsigned u1 = (u0 ^ 128) + 4 * 3584, u2 = u0 + 8 * 3584, u3 = u1 + 8 * 3584;
    const char* xb = ws + WS_H + (size_t)(rt * 8 + 2 * lw) * 16384;
    const char* wb = w1 + cb * 256 + lw * (16 * 3584);
    const unsigned ldx = lds0 + lw * 4096, ldw = lds0 + 16384 + lw * 4096;
#define K3_GX(S, V, JJ) glds16(V, xb + (((S) + rot) & 7) * 2048, ldx + ((S) & 3) * 32768 + (JJ) * 1024)
#define K3_GW(S, V, JJ) glds16(V, wb + (((S) + rot) & 7) * (64 * 3584), ldw + ((S) & 3) * 32768 + (JJ) * 1024)
#define K3_ST8(S) K3_GX(S, v0, 0); K3_GW(S, u0, 0); K3_GX(S, v2, 2); K3_GW(S, u1, 1); \
                  K3_GX(S, v1, 1); K3_GW(S, u2, 2); K3_GX(S, v3, 3); K3_GW(S, u3, 3)
    K3_ST8(0); K3_ST8(1);
    TS(1);
#define K3_SV 1
#define K3_E 0
#define K3_SL 1
    u32x2 sp[8];
    float mean, var, mb, r1;
#pragma unroll
    for (int s = 0; s < 8; ++s) {
      if (s == K3_SL + 1 || s == K3_SL + 2) wait_vm<16>(); else if (s < 7) wait_vm<8>(); else wait_vm<WK3T / 2>();
      BAR();
      MEMF();
      if (s == 0) TS(2);
      if (s == 4) TS(3);
      SB0();
      if (s < 6) { K3_ST8(s + 2); }
      if (s >= 6) {
        const char* p = w2 + (1792 - 112 * WK3T + (idx * 4 + lw) * WK3T) * 1024;
#pragma unroll
        for (int j = (s == 6 ? 0 : WK3T / 2); j < (s == 6 ? WK3T / 2 : WK3T); ++j) glds16(v0, p + j * 1024, ldx);
      }
      SB0();
      if (s == K3_SL) {
        const char* sb = ws + WS_ST + (size_t)(rt * 8 + 2 * lw) * 2048;
#pragma unroll
        for (int j = 0; j < 8; ++j) gld8(sp[j], g * 1024 + i * 8, sb + j * 128);
      }
      if (s == K3_SL + 3) {
        asm volatile("" : "+v"(sp[0]), "+v"(sp[1]), "+v"(sp[2]), "+v"(sp[3]), "+v"(sp[4]), "+v"(sp[5]), "+v"(sp[6]), "+v"(sp[7]));
        f32x2 sv = __builtin_bit_cast(f32x2, sp[0]);
#pragma unroll
        for (int j = 1; j < 8; ++j) sv += __builtin_bit_cast(f32x2, sp[j]);
        const float sv0 = sv[0], sv1 = sv[1];
        auto p1 = __builtin_amdgcn_permlane16_swap(__builtin_bit_cast(unsigned, sv0), __builtin_bit_cast(unsigned, sv0), false, false);
        auto p2 = __builtin_amdgcn_permlane16_swap(__builtin_bit_cast(unsigned, sv1), __builtin_bit_cast(unsigned, sv1), false, false);
        const unsigned p10 = p1[0], p11 = p1[1], p20 = p2[0], p21 = p2[1];
        mean = (__builtin_bit_cast(float, p10) + __builtin_bit_cast(float, p11)) * (1.0f / 512.0f);
        var = fmaxf((__builtin_bit_cast(float, p20) + __builtin_bit_cast(float, p21)) * (1.0f / 512.0f) - mean * mean, 0.f);
        asm volatile("" : "+v"(mean), "+v"(var));
      }
      if (s == K3_SL + 4) {
        mb = bfr(mean);
        r1 = __builtin_amdgcn_rcpf(bfr(__builtin_amdgcn_sqrtf(bfr(bfr(var) + 1e-5f))));
        asm volatile("" : "+v"(mb), "+v"(r1));
      }
      if (s == K3_SL + 5) {
        const float mn2 = bfr((mean - mb) * r1);
        const float var2 = var * r1 * r1;
        const float r2 = __builtin_amdgcn_rcpf(bfr(__builtin_amdgcn_sqrtf(bfr(bfr(var2) + 1e-5f))));
        if ((g & 1) == 0) *(f32x2*)(lds + K3_ST + (32 * lw + 8 * g + i) * 8) = (f32x2){r1 * r2, (mb * r1 + mn2) * r2};
      }
      SB0();
    }
    TS(4);
    WAIT_L0();
    BAR();
    wait_vm<0>();
    TS_END(3, 5, ws);
    return;
  }

  const int rg = w & 3, nh = w >> 2, kf = rg >> 1, tx = 2 * (rg & 1);
  u32x2 bb[4];
#pragma unroll
  for (int e = 0; e < 4; ++e) gld8(bb[e], g * 8, b1 + cb * 256 + nh * 128 + (e ^ tx) * 32);
  int xa[2], wa[2][4];
  xa[0] = rg * 4096 + kf * 1024 + l * 16; xa[1] = xa[0] ^ 1024;
  {
    const int wr = 16384 + (kf * 32 + 4 * g + (i >> 2)) * 256 + (i & 3) * 8;
    const int f = 4 * (g & 1) + (i >> 2);
#pragma unroll
    for (int e = 0; e < 4; ++e) { wa[0][e] = wr + (((4 * nh + (e ^ tx)) ^ f) * 32); wa[1][e] = wa[0][e] ^ 8192; }
  }
  const u32x4 onesu = {0x3f803f80u, 0x3f803f80u, 0x3f803f80u, 0x3f803f80u};
  const bf16x8 ones = as_bf8(onesu);
  f32x4 acc[2][4], c0 = z4, c1 = z4;
#pragma unroll
  for (int m = 0; m < 2; ++m)
#pragma unroll
    for (int e = 0; e < 4; ++e) acc[m][e] = z4;
  bf16x8 fx[2][2], fw[2][4];
#define K3_HALF(C, RD, SLOT)                                                                                   \
  SB0();                                                                                                       \
  if ((C) == 0 && s == 7) {                                                                                    \
    c0 = mfma32(fw[C][0], ones, c0);                                                                           \
    c1 = mfma32(fw[C][1], ones, c1);                                                                           \
  }                                                                                                            \
  SB0();                                                                                                       \
  if (RD) fx[(C) ^ 1][0] = lds_b128(lds, xa[(C) ^ 1] + (SLOT) * 32768);                                        \
  SB0();                                                                                                       \
  acc[0][0] = mfma32(fw[C][0], fx[C][0], acc[0][0]);                                                           \
  SB0();                                                                                                       \
  if (RD) fw[(C) ^ 1][0] = tr_frag(lds, wa[(C) ^ 1][0] + (SLOT) * 32768, 4096);                                \
  SB0();                                                                                                       \
  acc[1][0] = mfma32(fw[C][0], fx[C][1], acc[1][0]);                                                           \
  SB0();                                                                                                       \
  if (RD) fx[(C) ^ 1][1] = lds_b128(lds, xa[(C) ^ 1] + (SLOT) * 32768 + 2048);                                 \
  SB0();                                                                                                       \
  acc[0][1] = mfma32(fw[C][1], fx[C][0], acc[0][1]);                                                           \
  SB0();                                                                                                       \
  if (RD) fw[(C) ^ 1][1] = tr_frag(lds, wa[(C) ^ 1][1] + (SLOT) * 32768, 4096);                                \
  SB0();                                                                                                       \
  acc[1][1] = mfma32(fw[C][1], fx[C][1], acc[1][1]);                                                           \
  SB0();                                                                                                       \
  if (RD) fw[(C) ^ 1][2] = tr_frag(lds, wa[(C) ^ 1][2] + (SLOT) * 32768, 4096);                                \
  SB0();                                                                                                       \
  acc[0][2] = mfma32(fw[C][2], fx[C][0], acc[0][2]);                                                           \
  SB0();                                                                                                       \
  if (RD) fw[(C) ^ 1][3] = tr_frag(lds, wa[(C) ^ 1][3] + (SLOT) * 32768, 4096);                                \
  SB0();                                                                                                       \
  acc[1][2] = mfma32(fw[C][2], fx[C][1], acc[1][2]);                                                           \
  acc[0][3] = mfma32(fw[C][3], fx[C][0], acc[0][3]);                                                           \
  acc[1][3] = mfma32(fw[C][3], fx[C][1], acc[1][3]);                                                           \
  if ((C) == 0 && s < 7) {                                                                                     \
    c0 = mfma32(fw[C][0], ones, c0);                                                                           \
    c1 = mfma32(fw[C][1], ones, c1);                                                                           \
  }                                                                                                            \
  SB0();
  TS(1);
  BAR();
  MEMF();
  TS(2);
  fx[0][0] = lds_b128(lds, xa[0]);
  fw[0][0] = tr_frag(lds, wa[0][0], 4096);
  fx[0][1] = lds_b128(lds, xa[0] + 2048);
  fw[0][1] = tr_frag(lds, wa[0][1], 4096);
  fw[0][2] = tr_frag(lds, wa[0][2], 4096);
  fw[0][3] = tr_frag(lds, wa[0][3], 4096);
#pragma unroll
  for (int s = 0; s < 8; ++s) {
    K3_HALF(0, true, s & 3);
    if (s == 7) {
      if (i == 0) {
        *(f32x4*)(lds + K3_CS + nh * 512 + kf * 256 + tx * 64 + g * 16) = c0;
        *(f32x4*)(lds + K3_CS + nh * 512 + kf * 256 + tx * 64 + 64 + g * 16) = c1;
      }
    }
    if (s < 7) {
      BAR(); MEMF();
      if (s == 3) TS(3);
      K3_HALF(1, true, (s + 1) & 3);
    }
  }
  WAIT_L0();
  BAR();
  MEMF();
  f32x4 cs[4], cs2[4];
#pragma unroll
  for (int e = 0; e < 4; ++e) {
    const int ca = K3_CS + nh * 512 + (e ^ tx) * 64 + g * 16;
    cs[e] = *(const f32x4*)(lds + ca);
    cs2[e] = *(const f32x4*)(lds + ca + 256);
  }
  f32x2 scm[2];
#pragma unroll
  for (int m = 0; m < 2; ++m) scm[m] = *(const f32x2*)(lds + K3_ST + (32 * rg + 16 * m + i) * 8);
  SB0();
#pragma unroll
  for (int m = 0; m < 2; ++m)
#pragma unroll
    for (int e = 0; e < 4; ++e) acc[m][e] = mfma32(fw[1][e], fx[1][m], acc[m][e]);
  TS(4);
  asm volatile("s_waitcnt vmcnt(0)" : "+v"(bb[0]), "+v"(bb[1]), "+v"(bb[2]), "+v"(bb[3])::"memory");
  f32x2 gc1 = {-0.044715f * 2.302208198f, -0.044715f * 2.302208198f}, gc2 = {-2.302208198f, -2.302208198f}, gc3 = {1.0f, 1.0f};
  asm volatile("" : "+s"(gc1), "+v"(gc2), "+s"(gc3));
#pragma unroll
  for (int e = 0; e < 4; ++e) cs[e] += cs2[e];
#pragma unroll
  for (int m = 0; m < 2; ++m) {
    const f32x2 sc = scm[m];
#pragma unroll
    for (int t = 0; t < 2; ++t) {
      u32x4 o;
#pragma unroll
      for (int e2 = 0; e2 < 2; ++e2) {
        const int e = 2 * t + e2;
#pragma unroll
        for (int r = 0; r < 2; ++r) {
          const unsigned bu = bb[e][r];
          const f32x2 bv = {bflo(bu), bfhi(bu)};
          const f32x2 av = {acc[m][e][2 * r], acc[m][e][2 * r + 1]}, cv = {cs[e][2 * r], cs[e][2 * r + 1]};
          const f32x2 tt = __builtin_elementwise_fma(av, (f32x2){sc[0], sc[0]}, __builtin_elementwise_fma((f32x2){-sc[1], -sc[1]}, cv, bv));
          const f32x2 u = tt * __builtin_elementwise_fma(tt * tt, gc1, gc2);
          const f32x2 ex = {__builtin_amdgcn_exp2f(u[0]), __builtin_amdgcn_exp2f(u[1])};
          const f32x2 dn = ex + gc3;
          const f32x2 gl = tt * (f32x2){__builtin_amdgcn_rcpf(dn[0]), __builtin_amdgcn_rcpf(dn[1])};
          o[2 * e2 + r] = pk2(gl[0], gl[1]);
        }
      }
      gst16(ws + WS_HM + (size_t)((rt * 8 + rg * 2 + m) * 56 + cb * 4 + nh * 2 + (t ^ (rg & 1))) * 1024 + l * 16, o);
    }
  }
  TS_END(3, 5, ws);
}

static constexpr int K4_LDS = 7 * 16384;

__global__ __launch_bounds__(512, 2) void k4_down(const char* __restrict__ w2, const char* __restrict__ b2,
                                                   const char* __restrict__ ws, char* __restrict__ out) {
  __shared__ __attribute__((aligned(16))) char lds[K4_LDS];
  TS_DECL
  asm volatile("" ::"s"(w2), "s"(b2), "s"(ws), "s"(out));
  const int tid = threadIdx.x, l = tid & 63, i = l & 15, g = l >> 4;
  const int w = __builtin_amdgcn_readfirstlane(tid >> 6);
  const int b = blockIdx.x, xcd = b & 7, idx = b >> 3;
  const int rt = xcd * 4 + (idx >> 3), q = idx & 7;
  const f32x4 z4 = {0.f, 0.f, 0.f, 0.f};

  if (w >= 4) {
    const int lw = w - 4;
    const int rot = idx >> 3;
    const unsigned lds0 = (unsigned)(uintptr_t)(LDS_AS char*)lds;
    const unsigned v0 = l * 16;
    const unsigned vw = (l >> 3) * 1024 + ((((l >> 1) & 3) ^ ((l >> 4) & 3)) * 32) + (l & 1) * 16;
    const char* xb = ws + WS_HM + (size_t)(rt * 4 + lw) * (56 * 1024);
    const char* wb = w2 + q * 128 + lw * 16384;
    const unsigned ld = lds0 + lw * 2048;
#define K4_ST(XB, WB, U)                                                                                       \
  do {                                                                                                         \
    glds16(v0, (XB) + (U) * 2048, ld + (U) * 16384);                                                           \
    glds16(vw, (WB) + (U) * 65536, ld + (U) * 16384 + 8192);                                                   \
    glds16(v0, (XB) + (U) * 2048 + 1024, ld + (U) * 16384 + 1024);                                             \
    glds16(vw, (WB) + (U) * 65536 + 8192, ld + (U) * 16384 + 8192 + 1024);                                     \
  } while (0)
    const char* xc = xb + rot * (7 * 2048);
    const char* wc = wb + rot * (7 * 65536);
#define X4_D 4
#pragma unroll
    for (int u = 0; u < X4_D; ++u) K4_ST(xc, wc, u);
    TS(1);
#pragma unroll 1
    for (int o = 0; o < 4; ++o) {
      const int nx = (o + 1 + rot) & 3;
      const char* xn = xb + nx * (7 * 2048);
      const char* wn = wb + nx * (7 * 65536);
#pragma unroll
      for (int u = 0; u < 7; ++u) {
        if (u < 8 - X4_D) wait_vm<4 * (X4_D - 1)>();
        else if (o < 3) wait_vm<4 * (X4_D - 1)>();
        else if (u == 6) wait_vm<0>();
        else if (u == 5) wait_vm<4>();
        else if (u == 4) wait_vm<8>();
        else wait_vm<12>();
        BAR();
        MEMF();
        if (u < 7 - X4_D) K4_ST(xc, wc, u + X4_D);
        else if (o < 3) K4_ST(xn, wn, u + X4_D - 7);
      }
      xc = xn; wc = wn;
    }
    TS(2);
    BAR();
    TS_END(4, 3, ws);
    return;
  }

  const int mh = w & 1, nh = w >> 1;
  u32x4 hf[2];
  u32x2 bb[2];
#pragma unroll
  for (int m = 0; m < 2; ++m) gld16(hf[m], l * 16, ws + WS_H + (size_t)((rt * 4 + 2 * mh + m) * 16 + 2 * q + nh) * 1024);
#pragma unroll
  for (int e = 0; e < 2; ++e) gld8(bb[e], g * 8, b2 + q * 128 + nh * 64 + e * 32);
  const int xa = mh * 4096 + l * 16;
  int wa[2];
  {
    const int wr = 8192 + (4 * g + (i >> 2)) * 128 + (i & 3) * 8;
    const int f = (2 * g + (i >> 3)) & 3;
    wa[0] = wr + (((2 * nh) ^ f) * 32); wa[1] = wa[0] ^ 32;
  }
  f32x4 acc[2][2] = {{z4, z4}, {z4, z4}};
  bf16x8 fx[2][2][2], fw[2][2][2];
#define K4_RD(B, U)                                                                                            \
  _Pragma("unroll") for (int ks = 0; ks < 2; ++ks) {                                                           \
    fx[B][ks][0] = lds_b128(lds, xa + (U) * 16384 + ks * 1024);                                                \
    fw[B][ks][0] = tr_frag(lds, wa[0] + (U) * 16384 + ks * 4096, 2048);                                        \
    fx[B][ks][1] = lds_b128(lds, xa + (U) * 16384 + ks * 1024 + 2048);                                         \
    fw[B][ks][1] = tr_frag(lds, wa[1] + (U) * 16384 + ks * 4096, 2048);                                        \
  }
#define K4_MM(B)                                                                                               \
  _Pragma("unroll") for (int ks = 0; ks < 2; ++ks) {                                                           \
    acc[0][0] = mfma32(fw[B][ks][0], fx[B][ks][0], acc[0][0]);                                                 \
    acc[1][0] = mfma32(fw[B][ks][0], fx[B][ks][1], acc[1][0]);                                                 \
    acc[0][1] = mfma32(fw[B][ks][1], fx[B][ks][0], acc[0][1]);                                                 \
    acc[1][1] = mfma32(fw[B][ks][1], fx[B][ks][1], acc[1][1]);                                                 \
  }
  TS(1);
  BAR();
  MEMF();
  K4_RD(0, 0);
#pragma unroll 1
  for (int o = 0; o < 2; ++o) {
#pragma unroll
    for (int u = 0; u < 14; ++u) {
      BAR();
      MEMF();
      SB0();
      K4_RD((u & 1) ^ 1, (u + 1) % 7);
      SB0();
      K4_MM(u & 1);
      SB0();
    }
  }
  TS(2);
  asm volatile("s_waitcnt vmcnt(0)" : "+v"(hf[0]), "+v"(hf[1]), "+v"(bb[0]), "+v"(bb[1])::"memory");
#pragma unroll
  for (int m = 0; m < 2; ++m) {
    unsigned pk[2][2];
#pragma unroll
    for (int e = 0; e < 2; ++e) {
      float ov[4];
#pragma unroll
      for (int r = 0; r < 4; ++r) {
        const unsigned hu = hf[m][2 * e + (r >> 1)], bu = bb[e][r >> 1];
        const float hval = (r & 1) ? bfhi(hu) : bflo(hu);
        const float bval = (r & 1) ? bfhi(bu) : bflo(bu);
        ov[r] = hval + bfr(bfr(acc[m][e][r]) + bval);
      }
      pk[e][0] = pk2(ov[0], ov[1]); pk[e][1] = pk2(ov[2], ov[3]);
    }
    auto s0 = __builtin_amdgcn_permlane16_swap(pk[0][0], pk[1][0], false, false);
    auto s1 = __builtin_amdgcn_permlane16_swap(pk[0][1], pk[1][1], false, false);
    const u32x4 o = {s0[0], s1[0], s0[1], s1[1]};
    const int row = rt * 64 + mh * 32 + m * 16 + i;
    gst16(out + (size_t)row * 1024 + q * 128 + nh * 64 + (g & 1) * 32 + (g >> 1) * 16, o);
  }
  TS_END(4, 3, ws);
}

__global__ __launch_bounds__(768, 3) void k1_qkv(const char* __restrict__ x, const char* __restrict__ wqkv,
                                                  const char* __restrict__ wo, const char* __restrict__ w1,
                                                  const char* __restrict__ w2, char* __restrict__ ws) {
  __shared__ __attribute__((aligned(16))) char lds[K1_LDS];
  TS_DECL
  asm volatile("" ::"s"(x), "s"(wqkv), "s"(wo), "s"(w1), "s"(w2), "s"(ws));
  const int tid = threadIdx.x, l = tid & 63, i = l & 15, g = l >> 4;
  const int w = __builtin_amdgcn_readfirstlane(tid >> 6);
  const int b = blockIdx.x, xcd = b & 7, idx = b >> 3;
  const int rt = (xcd >> 1) * 4 + (idx >> 3), hp = (xcd & 1) * 8 + (idx & 7);
  const f32x4 z4 = {0.f, 0.f, 0.f, 0.f};

  if (w >= 8) {
    const int lw = w - 8;
    const int rot = ((idx & 7) + 2 * (idx >> 3)) & 7;
    const unsigned lds0 = (unsigned)(uintptr_t)(LDS_AS char*)lds;
    unsigned vx[4], vw[3];
#pragma unroll
    for (int jj = 0; jj < 4; ++jj)
      vx[jj] = (32 * lw + 8 * jj + (l >> 3)) * 1024 + (((l & 7) ^ ((jj & 1) * 4 + (l >> 4))) * 16);
#pragma unroll
    for (int jj = 0; jj < 3; ++jj) {
      const int n = 64 * (3 * lw + jj) + l, row = n / 12, c = n - 12 * row;
      vw[jj] = row * 3072 + (c >> 2) * 1024 + ((((c >> 1) & 1) ^ ((row >> 3) & 1)) * 32) + (c & 1) * 16;
    }
    const char* xb = x + (size_t)rt * 131072;
    const char* wb = wqkv + hp * 64;
    const unsigned ldx = lds0 + lw * 4096, ldw = lds0 + K1_WOFF + lw * 3072;
#define K1_GX(S, JJ) glds16(vx[JJ], xb + (((S) + rot) & 7) * 128, ldx + ((S) & 3) * 16384 + (JJ) * 1024)
#define K1_GW(S, JJ) glds16(vw[JJ], wb + (size_t)(((S) + rot) & 7) * 196608, ldw + ((S) & 3) * 12288 + (JJ) * 1024)
#pragma unroll
    for (int s = 0; s < 2; ++s) {
      K1_GX(s, 0); K1_GW(s, 0); K1_GX(s, 1); K1_GW(s, 1); K1_GX(s, 2); K1_GW(s, 2); K1_GX(s, 3);
    }
    TS(1);
#pragma unroll
    for (int s = 0; s < 8; ++s) {
      if (s < 7) wait_vm<7>(); else wait_vm<W1K ? 4 : 0>();
      BAR();
      MEMF();
      if (s == 0) TS(2);
      if (s == 4) TS(3);
      SB0();
      if (s == 6) {
        const char* p = wo + (idx * 4 + lw) * 4096;
#pragma unroll
        for (int j = 0; j < 4; ++j) glds16(l * 16, p + j * 1024, ldx);
      }
      if (s == 7) {
        const int gw = min(b * 4 + lw, 895) * 2048;
        glds16(l * 16, w1 + gw, ldx); glds16(l * 16, w1 + gw + 1024, ldx);
        glds16(l * 16, w2 + gw, ldx); glds16(l * 16, w2 + gw + 1024, ldx);
      }
      if (s < 6) {
        K1_GX(s + 2, 0); K1_GW(s + 2, 0); K1_GX(s + 2, 1); K1_GW(s + 2, 1); K1_GX(s + 2, 2); K1_GW(s + 2, 2); K1_GX(s + 2, 3);
      }
      SB0();
    }
    TS(4);
    BAR();
    BAR();
    wait_vm<0>();
    TS_END(0, 5, ws);
    return;
  }

  const int rg = w & 3, h = w >> 2, kf = rg & 1;
  const u32x4 onesu = {0x3f803f80u, 0x3f803f80u, 0x3f803f80u, 0x3f803f80u};
  const bf16x8 ones = as_bf8(onesu);
  int xa[2][2], wa[2];
  {
    const int xr = (32 * rg + 16 * h + i) * 128 + ((g ^ ((i >> 1) & 7)) * 16);
    xa[0][0] = xr ^ (kf * 64); xa[0][1] = xa[0][0] ^ 2048;
    xa[1][0] = xa[0][0] ^ 64;  xa[1][1] = xa[1][0] ^ 2048;
    const int wr = K1_WOFF + (8 * g + (i >> 2)) * 192 + ((h ^ (g & 1)) * 32) + (i & 3) * 8;
    wa[0] = wr + kf * 6144; wa[1] = wr + (kf ^ 1) * 6144;
  }
  f32x4 aq[2] = {z4, z4}, ak[2] = {z4, z4}, av[2] = {z4, z4}, cq = z4, ck = z4, cv = z4, gs = z4, rs1 = z4;
  bf16x8 fx[2][2], fw[2][3];
#define K1_HALF(C, RD, SLOT, CS)                                                                               \
  SB0();                                                                                                       \
  if (RD) fx[(C) ^ 1][0] = lds_b128(lds, xa[(C) ^ 1][0] + (SLOT) * 16384);                                     \
  SB0();                                                                                                       \
  aq[0] = mfma32(fw[C][0], fx[C][0], aq[0]);                                                                   \
  SB0();                                                                                                       \
  if (RD) fw[(C) ^ 1][0] = tr_frag(lds, wa[(C) ^ 1] + (SLOT) * 12288, 768);                                    \
  SB0();                                                                                                       \
  aq[1] = mfma32(fw[C][0], fx[C][1], aq[1]);                                                                   \
  SB0();                                                                                                       \
  if (RD) fx[(C) ^ 1][1] = lds_b128(lds, xa[(C) ^ 1][1] + (SLOT) * 16384);                                     \
  SB0();                                                                                                       \
  ak[0] = mfma32(fx[C][0], fw[C][1], ak[0]);                                                                   \
  SB0();                                                                                                       \
  if (RD) fw[(C) ^ 1][1] = tr_frag(lds, wa[(C) ^ 1] + (SLOT) * 12288 + 64, 768);                               \
  SB0();                                                                                                       \
  ak[1] = mfma32(fx[C][1], fw[C][1], ak[1]);                                                                   \
  SB0();                                                                                                       \
  if (RD) fw[(C) ^ 1][2] = tr_frag(lds, wa[(C) ^ 1] + (SLOT) * 12288 + 128, 768);                              \
  SB0();                                                                                                       \
  av[0] = mfma32(fx[C][0], fw[C][2], av[0]);                                                                   \
  av[1] = mfma32(fx[C][1], fw[C][2], av[1]);                                                                   \
  gs = mfma32(fx[C][0], fx[C][0], gs);                                                                         \
  rs1 = mfma32(ones, fx[C][0], rs1);                                                                           \
  if (CS) {                                                                                                    \
    cq = mfma32(fw[C][0], ones, cq);                                                                           \
    ck = mfma32(ones, fw[C][1], ck);                                                                           \
    cv = mfma32(ones, fw[C][2], cv);                                                                           \
  }                                                                                                            \
  SB0();
  if (w == 7 && b < 17) gst16(ws + WS_KVF + b * 1024 + l * 16, (u32x4){~0u, ~0u, ~0u, ~0u});
  if (w == 6 && idx < CODE_KB) {
    const char* cp;
    int co;
    asm volatile("s_getpc_b64 %0\n\ts_mov_b32 %1, k2_attn@rel32@lo+4" : "=s"(cp), "=s"(co));
    cp += co;
    glds16(l * 16, cp + idx * 1024, (unsigned)(uintptr_t)(LDS_AS char*)lds + K1_KV);
  }
  TS(1);
  BAR();
  MEMF();
  TS(2);
  fx[0][0] = lds_b128(lds, xa[0][0]);
  fw[0][0] = tr_frag(lds, wa[0], 768);
  fx[0][1] = lds_b128(lds, xa[0][1]);
  fw[0][1] = tr_frag(lds, wa[0] + 64, 768);
  fw[0][2] = tr_frag(lds, wa[0] + 128, 768);
#pragma unroll
  for (int s = 0; s < 8; ++s) {
    K1_HALF(0, true, s & 3, true);
    if (s < 7) {
      BAR(); MEMF();
      if (s == 3) TS(3);
      K1_HALF(1, true, (s + 1) & 3, false);
    }
  }
  gs = mfma32(fx[1][0], fx[1][0], gs);
  rs1 = mfma32(ones, fx[1][0], rs1);
  SB0();
  aq[0] = mfma32(fw[1][0], fx[1][0], aq[0]);
  aq[1] = mfma32(fw[1][0], fx[1][1], aq[1]);
  ak[0] = mfma32(fx[1][0], fw[1][1], ak[0]);
  ak[1] = mfma32(fx[1][1], fw[1][1], ak[1]);
  av[0] = mfma32(fx[1][0], fw[1][2], av[0]);
  av[1] = mfma32(fx[1][1], fw[1][2], av[1]);
  {
    const float d01 = (i & 1) ? gs[1] : gs[0], d23 = (i & 1) ? gs[3] : gs[2];
    const float s2 = (i & 2) ? d23 : d01;
    const float mean = rs1[0] * (1.0f / 512.0f);
    const float var = fmaxf(s2 * (1.0f / 512.0f) - mean * mean, 0.f);
    const float rs = __builtin_amdgcn_rsqf(var + 1e-5f);
    if (g == (i >> 2)) *(f32x2*)(lds + K1_ST + (32 * rg + 16 * h + i) * 8) = (f32x2){rs, mean * rs};
  }
  TS(4);
  if (i == 0) *(f32x4*)(lds + K1_CS + w * 192 + g * 16) = cq;
  if (g == 0) {
    *(float*)(lds + K1_CS + w * 192 + 64 + i * 4) = ck[0];
    *(float*)(lds + K1_CS + w * 192 + 128 + i * 4) = cv[0];
  }
  wait_vm<0>();
  WAIT_L0();
  BAR();
  MEMF();
  cq += *(const f32x4*)(lds + K1_CS + (w ^ 1) * 192 + g * 16);
  const float csk = ck[0] + *(const float*)(lds + K1_CS + (w ^ 1) * 192 + 64 + i * 4);
  const float csv = cv[0] + *(const float*)(lds + K1_CS + (w ^ 1) * 192 + 128 + i * 4);
  u32x4 pq, pk, pv;
#pragma unroll
  for (int m = 0; m < 2; ++m) {
    const int rb = K1_ST + (32 * rg + 16 * (h ^ m)) * 8;
    const f32x2 sq = *(const f32x2*)(lds + rb + i * 8);
    const f32x4 s01 = *(const f32x4*)(lds + rb + g * 32);
    const f32x4 s23 = *(const f32x4*)(lds + rb + g * 32 + 16);
    float q[4], k[4], v[4];
#pragma unroll
    for (int r = 0; r < 4; ++r) {
      const float rs = r == 0 ? s01[0] : (r == 1 ? s01[2] : (r == 2 ? s23[0] : s23[2]));
      const float mr = r == 0 ? s01[1] : (r == 1 ? s01[3] : (r == 2 ? s23[1] : s23[3]));
      q[r] = phi_p(__builtin_fmaf(aq[m][r], sq[0], __builtin_fmaf(-sq[1], cq[r], 1e-6f)));
      k[r] = phi_p(__builtin_fmaf(ak[m][r], rs, __builtin_fmaf(-mr, csk, 1e-6f)));
      v[r] = __builtin_fmaf(av[m][r], rs, -mr * csv);
    }
    pq[2 * m] = pk2(q[0], q[1]); pq[2 * m + 1] = pk2(q[2], q[3]);
    pk[2 * m] = pk2(k[0], k[1]); pk[2 * m + 1] = pk2(k[2], k[3]);
    pv[2 * m] = pk2(v[0], v[1]); pv[2 * m + 1] = pk2(v[2], v[3]);
  }
  const f32x4 kvt = mfma32(as_bf8(pk), as_bf8(pv), z4);
  const f32x4 zz = mfma32(as_bf8(pk), ones, z4);
  {
    char* pd = ws + WS_PQ + (size_t)(((rt * 4 + rg) * 2 + h) * 16 + hp) * 1024 + l * 16 + h * 8;
    gst8(pd, (u32x2){pq[0], pq[1]});
    gst8(pd + (1 - 2 * h) * 16384, (u32x2){pq[2], pq[3]});
  }
  *(f32x4*)(lds + K1_KV + w * 1024 + l * 16) = kvt;
  if (i == 0) *(f32x4*)(lds + K1_Z + w * 64 + g * 16) = zz;
  WAIT_L0();
  BAR();
  MEMF();
  if (w < 2) {
    f32x4 s = *(const f32x4*)(lds + K1_KV + w * 4096 + l * 16);
#pragma unroll
    for (int r = 1; r < 4; ++r) s += *(const f32x4*)(lds + K1_KV + w * 4096 + r * 1024 + l * 16);
    gst16(ws + WS_KVP + (size_t)(rt * 32 + 2 * hp + w) * 1024 + l * 16, __builtin_bit_cast(u32x4, s));
  } else if (w == 2 && l < 8) {
    f32x4 s = *(const f32x4*)(lds + K1_Z + (l >> 2) * 256 + (l & 3) * 16);
#pragma unroll
    for (int r = 1; r < 4; ++r) s += *(const f32x4*)(lds + K1_Z + (l >> 2) * 256 + r * 64 + (l & 3) * 16);
    gst16(ws + WS_ZP + (size_t)(rt * 32 + 2 * hp) * 64 + l * 16, __builtin_bit_cast(u32x4, s));
  }
  TS_END(0, 5, ws);
}

extern "C" void kernel_launch(void* const* d_in, const int* in_sizes, int n_in, void* d_out, int out_size,
                              void* d_ws, size_t ws_size, hipStream_t stream) {
  const char* x = (const char*)d_in[0];
  const char* wqkv = (const char*)d_in[1];
  const char* wo = (const char*)d_in[2];
  const char* w1 = (const char*)d_in[4];
  const char* b1 = (const char*)d_in[5];
  const char* w2 = (const char*)d_in[6];
  const char* b2 = (const char*)d_in[7];
  char* ws = (char*)d_ws;
  k1_qkv<<<256, 768, 0, stream>>>(x, wqkv, wo, w1, w2, ws);
  k2_attn<<<256, 768, 0, stream>>>(x, wo, w1, ws);
  k3_up<<<WK3S ? 256 : 224, 768, 0, stream>>>(w1, b1, w2, ws);
  k4_down<<<256, 512, 0, stream>>>(w2, b2, ws, (char*)d_out);
}
```
